```python
import torch

def _softmax(x, axis=-1):
    return torch.softmax(x, dim=axis)
def _gelu(x):
    return torch.nn.functional.gelu(x, approximate="tanh")
def _silu(x):
    return torch.nn.functional.silu(x)
def _sigmoid(x):
    return torch.sigmoid(x)
def _layernorm(x, axis=-1, eps=1e-5):
    m = x.mean(dim=axis, keepdim=True)
    v = x.var(dim=axis, keepdim=True, unbiased=False)
    return (x - m) / torch.sqrt(v + eps)
def _scatter_add(tgt, idx, src, axis=0):
    return tgt.clone().index_add_(axis, idx, src)
def _segment_sum(src, seg, n):
    out = torch.zeros((n,) + src.shape[1:], dtype=src.dtype)
    return out.index_add_(0, seg, src)
def _pad(x, pw):
    flat = tuple(v for pair in reversed(pw) for v in pair)
    return torch.nn.functional.pad(x, flat)

kernel_name = 'interleave_layers__sliding_block__mha_block__prompt'

def setup_inputs():

    _w = (lambda a: (lambda t: t.to(torch.bfloat16) if t.is_floating_point() else t)(torch.as_tensor(a)))
    def _A_setup():
        B, S, D = 1, 2048, 640
        return {
            "x": _w((lambda *s: torch.randn(*s, dtype=torch.bfloat16) * 0.05)(B, S, D)),
            "wqkv": _w((lambda *s: torch.randn(*s, dtype=torch.bfloat16) * 0.05)(D, 3 * D)),
            "wo": _w((lambda *s: torch.randn(*s, dtype=torch.bfloat16) * 0.05)(D, D)),
        }
    _A_in = _A_setup()

    def _B_setup():
        B, S, D, H = 1, 2048, 640, 32
        return {
            "x": _w((lambda *s: torch.randn(*s, dtype=torch.bfloat16) * 0.05)(B, S, D)),
            "wqkv": _w((lambda *s: torch.randn(*s, dtype=torch.bfloat16) * 0.05)(D, 3 * D)),
            "wo": _w((lambda *s: torch.randn(*s, dtype=torch.bfloat16) * 0.05)(D, D)),
        }
    _B_in = _B_setup()

    return {
        "A_x": _A_in["x"],
        "A_wqkv": _A_in["wqkv"],
        "A_wo": _A_in["wo"],
        "B_x": _B_in["x"],
        "B_wqkv": _B_in["wqkv"],
        "B_wo": _B_in["wo"],
    }

def reference(A_x, A_wqkv, A_wo, B_x, B_wqkv, B_wo):
    def _A_ref(x, wqkv, wo):
        B, S, D = x.shape
        H = 32; Dh = D // H; W = 256
        qkv = (lambda x, *d: x.permute(*d))((x @ wqkv).reshape(B, S, 3, H, Dh), 2, 0, 3, 1, 4)
        q, k, v = qkv[0], qkv[1], qkv[2]
        scores = torch.einsum("bhqd,bhkd->bhqk", q, k) * (Dh ** -0.5)
        idx = torch.arange(S)
        mask = (idx[:, None] - idx[None, :] >= 0) & (idx[:, None] - idx[None, :] < W)
        scores = torch.where(mask[None, None], scores, -1e9)
        p = _softmax(scores, axis=-1)
        o = torch.einsum("bhqk,bhkd->bhqd", p, v)
        return (lambda x, *d: x.permute(*d))(o, 0, 2, 1, 3).reshape(B, S, D) @ wo

    def _B_ref(x, wqkv, wo):
        B, S, D = x.shape
        H = 32
        Dh = D // H
        qkv = (lambda x, *d: x.permute(*d))((x @ wqkv).reshape(B, S, 3, H, Dh), 2, 0, 3, 1, 4)
        q, k, v = qkv[0], qkv[1], qkv[2]
        scores = torch.einsum("bhqd,bhkd->bhqk", q, k) * (Dh ** -0.5)
        p = _softmax(scores, axis=-1)
        o = torch.einsum("bhqk,bhkd->bhqd", p, v)
        return (lambda x, *d: x.permute(*d))(o, 0, 2, 1, 3).reshape(B, S, D) @ wo

    x = A_x
    x = x + _A_ref(_layernorm(x, axis=-1), A_wqkv, A_wo)
    x = x + _B_ref(_layernorm(x, axis=-1), B_wqkv, B_wo)
    x = x + _A_ref(_layernorm(x, axis=-1), A_wqkv, A_wo)
    x = x + _B_ref(_layernorm(x, axis=-1), B_wqkv, B_wo)
    return x
```

```cpp
#define CODE_BYTES 30464
#define XRELAY 0
#include <hip/hip_runtime.h>
#include <hip/hip_bf16.h>
#include <stdint.h>

typedef __attribute__((ext_vector_type(8))) short s16x8;
typedef __attribute__((ext_vector_type(4))) short s16x4;
typedef __attribute__((ext_vector_type(16))) float f32x16;
typedef __attribute__((ext_vector_type(32))) float f32x32;
typedef __attribute__((ext_vector_type(4))) float f32x4;
typedef __attribute__((ext_vector_type(2))) float f32x2;
typedef __attribute__((ext_vector_type(4))) unsigned u32x4;
typedef __attribute__((ext_vector_type(2))) unsigned u32x2;
typedef __attribute__((ext_vector_type(2))) __bf16 bf16x2_t;
typedef unsigned long long u64;
#define LAS __attribute__((address_space(3)))
#define GAS __attribute__((address_space(1)))
typedef LAS char* lds_ptr;

#define DI __device__ __forceinline__
#define SBAR() __builtin_amdgcn_sched_barrier(0)
#define MFMA32(a, b, c) __builtin_amdgcn_mfma_f32_32x32x16_bf16((a), (b), (c), 0, 0, 0)
#define MFMA32K8(a, b, c) __builtin_amdgcn_mfma_f32_32x32x8bf16_1k((a), (b), (c), 0, 0, 0)
#define MFMA16(a, b, c) __builtin_amdgcn_mfma_f32_16x16x32_bf16((a), (b), (c), 0, 0, 0)
#define WG_BARRIER() asm volatile("s_waitcnt lgkmcnt(0)\n\ts_barrier" ::: "memory")
#define DRAIN() asm volatile("s_waitcnt vmcnt(0)" ::: "memory")

#define LAYOUT 0
#define TOUCH 0
constexpr int SEQ = 2048, DM = 640;
constexpr size_t ACT_BYTES = (size_t)SEQ * DM * 2;
constexpr size_t WQ_BYTES = (size_t)32 * 76800;
constexpr size_t WO_BYTES = (size_t)8 * 102400;
constexpr size_t ST_BYTES = (size_t)SEQ * 8 * 8;
constexpr size_t KVQ_BYTES = (size_t)SEQ * 48;
constexpr size_t KV_BYTES = (size_t)32 * 2 * KVQ_BYTES;
constexpr size_t OFF_EPOCH = 0;
constexpr size_t OFF_MBX = 256;
constexpr size_t OFF_MBO = OFF_MBX + 4 * 256 * 512;
constexpr size_t OFF_MBP = OFF_MBO + 4 * 256 * 512;
constexpr size_t OFF_MBW = OFF_MBP + 256 * 256;
constexpr size_t OFF_MBC = OFF_MBW + 256 * 128;
constexpr size_t OFF_MBKV = OFF_MBC + 256 * 128;
constexpr size_t OFF_CS = OFF_MBKV + 4 * 256 * 128;
constexpr size_t OFF_ST = OFF_CS + 2 * 16 * 16 * 512;
constexpr size_t OFF_WQ = OFF_ST + 4 * ST_BYTES;
constexpr size_t OFF_WO = OFF_WQ + 2 * WQ_BYTES;
constexpr size_t OFF_X = OFF_WO + 2 * WO_BYTES;
constexpr size_t OFF_O = OFF_X + 4 * ACT_BYTES;
constexpr size_t OFF_KV = OFF_O + 4 * ACT_BYTES;
constexpr float QSCALE = 0.22360679774997896f * 1.4426950408889634f;

constexpr int L_WO = 0;
constexpr int L_RING = 102400;
constexpr int L_CS = 151552;
constexpr int L_PROG = L_CS + 2048;
constexpr int L_WRDY = L_PROG + 2048;
constexpr int L_CSRDY = L_WRDY + 16;
constexpr int L_WORDY = L_CSRDY + 16;
constexpr int L_KVRDY = L_WORDY + 16;
constexpr int L_ADONE = L_KVRDY + 32;
constexpr int L_XCNT = L_ADONE + 8;
constexpr int L_KVOUT = L_XCNT + 8;
constexpr int L_PCNT = L_KVOUT + 8;
constexpr int L_BCNT = L_PCNT + 4;
constexpr int L_ACNT = L_BCNT + 4;
constexpr int L_TRDY = L_WRDY + 128;
constexpr int L_WODONE = L_WRDY + 192;
constexpr int L_QPROG = L_WRDY + 224;
constexpr int L_SINK = L_WRDY + 256;
constexpr int L_STG = 76800;
constexpr int LDS_BYTES = 163840;
constexpr float MASK_STEP = 1.2676506e30f;
constexpr unsigned SPIN_BUDGET = 1u << 20;

#define ST(i)
#define ST_FLUSH()

DI unsigned pk_bf16(float lo, float hi) {
    f32x2 v = {lo, hi};
    return __builtin_bit_cast(unsigned, __builtin_convertvector(v, bf16x2_t));
}
DI float bf_lo(unsigned u) { return __uint_as_float(u << 16); }
DI float bf_hi(unsigned u) { return __uint_as_float(u & 0xffff0000u); }
DI float dot_ones(unsigned u, float acc) {
    return __builtin_amdgcn_fdot2_f32_bf16(__builtin_bit_cast(bf16x2_t, u), __builtin_bit_cast(bf16x2_t, 0x3f803f80u), acc, false);
}

DI float dot_self(unsigned u, float acc) {
    return __builtin_amdgcn_fdot2_f32_bf16(__builtin_bit_cast(bf16x2_t, u), __builtin_bit_cast(bf16x2_t, u), acc, false);
}
template <typename T> DI T* uni(T* p) {
    const u64 v = (u64)p;
    return (T*)(((u64)(unsigned)__builtin_amdgcn_readfirstlane((unsigned)(v >> 32)) << 32) | (unsigned)__builtin_amdgcn_readfirstlane((unsigned)v));
}
template <int OFF> DI void dma16(const char* sbase, unsigned voff) {
    asm volatile("global_load_lds_dwordx4 %0, %1 offset:%2 sc1" :: "v"(voff), "s"(sbase), "i"(OFF) : "memory");
}
template <int OFF> DI void dma16p(const char* sbase, unsigned voff) {
    asm volatile("global_load_lds_dwordx4 %0, %1 offset:%2" :: "v"(voff), "s"(sbase), "i"(OFF) : "memory");
}
DI void set_m0(unsigned v) { asm volatile("s_mov_b32 m0, %0\n\ts_nop 0" :: "s"(v) : "memory"); }
template <int OFF> DI void gld16(u32x4& dst, const char* sbase, unsigned voff) {
    asm volatile("global_load_dwordx4 %0, %1, %2 offset:%3 sc1" : "=v"(dst) : "v"(voff), "s"(sbase), "i"(OFF) : "memory");
}
template <int OFF> DI void gld16p(u32x4& dst, const char* sbase, unsigned voff) {
    asm volatile("global_load_dwordx4 %0, %1, %2 offset:%3" : "=v"(dst) : "v"(voff), "s"(sbase), "i"(OFF) : "memory");
}
template <int OFF> DI void gld8(u32x2& dst, const char* sbase, unsigned voff) {
    asm volatile("global_load_dwordx2 %0, %1, %2 offset:%3 sc1" : "=v"(dst) : "v"(voff), "s"(sbase), "i"(OFF) : "memory");
}
DI void gst16(char* sbase, unsigned voff, u32x4 v) {
    asm volatile("global_store_dwordx4 %0, %1, %2 sc1\n\ts_nop 1" :: "v"(voff), "v"(v), "s"(sbase) : "memory");
}
DI void gst8(char* sbase, unsigned voff, u32x2 v) {
    asm volatile("s_nop 4\n\tglobal_store_dwordx2 %0, %1, %2 sc1" :: "v"(voff), "v"(v), "s"(sbase) : "memory");
}
DI void gst8_lo(char* sbase, unsigned voff, u32x2 v) {
    asm volatile("s_mov_b32 exec_hi, 0\n\tglobal_store_dwordx2 %0, %1, %2 sc1\n\ts_mov_b32 exec_hi, -1" :: "v"(voff), "v"(v), "s"(sbase) : "memory");
}
DI s16x8 lds_rd128(lds_ptr p) { return *(const LAS s16x8*)p; }
DI s16x4 lds_rd64(lds_ptr p) { return *(const LAS s16x4*)p; }
DI s16x4 lds_tr64(lds_ptr p) { return __builtin_amdgcn_ds_read_tr16_b64_v4i16((LAS s16x4*)p); }
DI unsigned lds_word(lds_ptr p) { return *(volatile LAS unsigned*)p; }
DI float half_swap_max(float m) {
    auto rr = __builtin_amdgcn_permlane32_swap(__float_as_uint(m), __float_as_uint(m), false, false);
    return fmaxf(__uint_as_float(rr[0]), __uint_as_float(rr[1]));
}
DI float quarter_swap_add(float m) {
    auto rr = __builtin_amdgcn_permlane16_swap(__float_as_uint(m), __float_as_uint(m), false, false);
    return __uint_as_float(rr[0]) + __uint_as_float(rr[1]);
}
DI float half_swap_add(float m) {
    auto rr = __builtin_amdgcn_permlane32_swap(__float_as_uint(m), __float_as_uint(m), false, false);
    return __uint_as_float(rr[0]) + __uint_as_float(rr[1]);
}

DI unsigned poll_mailbox(const char* sbase, unsigned voff, u64 tag, unsigned want, unsigned& budget) {
    sbase = uni(sbase);
    budget = __builtin_amdgcn_readfirstlane(budget);
    want = __builtin_amdgcn_readfirstlane(want);
    unsigned seen; u64 x;
    asm volatile("s_nop 4\n\ts_mov_b32 exec_lo, %[want]\n\ts_mov_b32 exec_hi, 0\n1:\n\tglobal_atomic_or_x2 %[x], %[voff], %[z], %[sb] sc0 sc1\n\ts_waitcnt vmcnt(0)\n\tv_cmp_eq_u64 vcc, %[tag], %[x]\n\t"
                 "s_cmp_eq_u32 vcc_lo, %[want]\n\ts_cbranch_scc1 2f\n\t"
                 "s_cmp_eq_u32 %[bud], 0\n\ts_cbranch_scc1 2f\n\ts_sub_u32 %[bud], %[bud], 1\n\ts_branch 1b\n2:\n\ts_mov_b64 exec, -1\n\ts_mov_b32 %[seen], vcc_lo"
                 : [x] "=&v"(x), [seen] "=&s"(seen), [bud] "+&s"(budget)
                 : [voff] "v"(voff), [sb] "s"(sbase), [tag] "s"(tag), [want] "s"(want), [z] "v"(0ull) : "vcc", "scc", "memory");
    return seen;
}
template <int SLEEP> DI void wait_lds(lds_ptr p, unsigned need, unsigned& budget) {
    budget = __builtin_amdgcn_readfirstlane(budget);
    need = __builtin_amdgcn_readfirstlane(need);
    unsigned t;
    asm volatile("1:\n\tds_read_b32 %[t], %[a]\n\ts_waitcnt lgkmcnt(0)\n\tv_cmp_gt_u32 vcc, %[need], %[t]\n\ts_cbranch_vccz 2f\n\t"
                 "s_cmp_eq_u32 %[bud], 0\n\ts_cbranch_scc1 2f\n\ts_sub_u32 %[bud], %[bud], 1\n\ts_sleep %[sl]\n\ts_branch 1b\n2:"
                 : [t] "=&v"(t), [bud] "+&s"(budget) : [a] "v"((unsigned)(uintptr_t)p), [need] "s"(need), [sl] "i"(SLEEP) : "vcc", "scc", "memory");
}
DI unsigned lds_arrive(lds_ptr p) {
    unsigned old, t;
    asm volatile("s_mov_b64 exec, 1\n\tds_add_rtn_u32 %[t], %[a], %[one]\n\ts_waitcnt lgkmcnt(0)\n\tv_readfirstlane_b32 %[o], %[t]\n\ts_mov_b64 exec, -1"
                 : [o] "=s"(old), [t] "=&v"(t) : [a] "v"((unsigned)(uintptr_t)p), [one] "v"(1u) : "memory");
    return old;
}
DI void publish(unsigned cnt, unsigned last, char* sbase, unsigned voff, u64 tag, unsigned mask) {
    unsigned old, t;
    asm volatile("s_waitcnt vmcnt(0)\n\ts_mov_b64 exec, 1\n\tds_add_rtn_u32 %[t], %[a], %[one]\n\ts_waitcnt lgkmcnt(0)\n\tv_readfirstlane_b32 %[o], %[t]\n\t"
                 "s_cmp_eq_u32 %[o], %[last]\n\ts_cbranch_scc0 1f\n\ts_mov_b32 exec_lo, %[mask]\n\tglobal_store_dwordx2 %[voff], %[tag], %[sb] sc1\n1:\n\ts_mov_b64 exec, -1"
                 : [o] "=&s"(old), [t] "=&v"(t)
                 : [a] "v"(cnt), [one] "v"(1u), [last] "s"(last), [sb] "s"(sbase), [voff] "v"(voff), [tag] "v"(tag), [mask] "s"(mask) : "scc", "memory");
}
DI void post_lds(lds_ptr p, unsigned v) { *(volatile LAS unsigned*)p = v; }

template <int WIN, int STEP> DI void img_dma(const char* src, unsigned dst, unsigned voff, int first, int end) {
#pragma unroll 1
    for (int p = first; p < end; p += STEP) {
        set_m0(dst + p * 1024);
        dma16<0>(src + p * 1024, voff);
        asm volatile("s_waitcnt vmcnt(%0)" :: "i"(WIN) : "memory");
    }
}

DI int fresh_lane() {
    int l = __builtin_amdgcn_mbcnt_hi(~0u, __builtin_amdgcn_mbcnt_lo(~0u, 0u));
    asm volatile("" : "+v"(l));
    return l;
}

DI u32x4 pack8(const f32x16& p, int s) {
    return (u32x4){pk_bf16(p[8 * s], p[8 * s + 1]), pk_bf16(p[8 * s + 2], p[8 * s + 3]), pk_bf16(p[8 * s + 4], p[8 * s + 5]), pk_bf16(p[8 * s + 6], p[8 * s + 7])};
}
DI float max16(const f32x16& p) {
    float a = fmaxf(fmaxf(p[0], p[1]), p[2]), b = fmaxf(fmaxf(p[3], p[4]), p[5]);
    a = fmaxf(fmaxf(a, p[6]), p[7]); b = fmaxf(fmaxf(b, p[8]), p[9]);
    a = fmaxf(fmaxf(a, p[10]), p[11]); b = fmaxf(fmaxf(b, p[12]), p[13]);
    a = fmaxf(fmaxf(a, p[14]), p[15]);
    return fmaxf(a, b);
}

DI f32x16 attn_exact(bool sliding, const char* kq, int qtile, s16x8 qm, s16x4 qt) {
    const int lane = fresh_lane(), r = lane & 31, h = lane >> 5;
    const int t0 = sliding ? (qtile > 8 ? qtile - 8 : 0) : 0, t1 = sliding ? qtile + 1 : 64;
    const int vq = r >> 2, vslot = (vq < 4 ? ((vq & 1) * 2 + (vq >> 1)) * 4 : 512 + (vq < 5 ? 0 : 4)) + (r & 3), vstr = vq < 4 ? 16 : 8;
    float m = -INFINITY;
    f32x16 o = f32x16{};
#pragma unroll 1
    for (int pass = 0; pass < 2; ++pass) {
#pragma unroll 1
        for (int t = t0; t < t1; ++t) {
            const char* kb = kq + (size_t)t * 1536;
            u32x4 km; u32x2 kt;
            gld16<0>(km, kb, r * 32 + h * 16);
            gld8<1024>(kt, kb, r * 16 + h * 8);
            asm volatile("s_waitcnt vmcnt(0)" : "+v"(km), "+v"(kt) :: "memory");
            f32x16 s = MFMA32(__builtin_bit_cast(s16x8, km), qm, f32x16{});
            s = MFMA32K8(__builtin_bit_cast(s16x4, kt), qt, s);
            if (sliding) {
#pragma unroll
                for (int i = 0; i < 16; ++i) {
                    const int dlt = (qtile - t) * 32 + r - ((i & 3) + 8 * (i >> 2) + 4 * h);
                    if (dlt < 0 || dlt >= 256) s[i] = -INFINITY;
                }
            }
            if (pass == 0) { m = fmaxf(m, max16(s)); continue; }
#pragma unroll
            for (int i = 0; i < 16; ++i) s[i] = __builtin_amdgcn_exp2f(s[i] - m);
            const __attribute__((address_space(1))) unsigned short* vb = (const __attribute__((address_space(1))) unsigned short*)(kb + KVQ_BYTES) + vslot;
#pragma unroll
            for (int ks = 0; ks < 2; ++ks) {
                s16x8 vf;
#pragma unroll
                for (int j = 0; j < 8; ++j)
                    vf[j] = (short)__hip_atomic_load(vb + (16 * ks + 8 * (j >> 2) + 4 * h + (j & 3)) * vstr, __ATOMIC_RELAXED, __HIP_MEMORY_SCOPE_AGENT);
                o = MFMA32(vf, __builtin_bit_cast(s16x8, pack8(s, ks)), o);
            }
        }
        if (pass == 0) m = half_swap_max(m);
    }
    return o;
}

DI f32x16 attn_full(s16x8 qm, s16x4 qtl, unsigned ka, unsigned kta, unsigned va, unsigned proga, unsigned rdya, unsigned rdyb, unsigned need,
                    unsigned& budget) {
    f32x16 o;
    need = __builtin_amdgcn_readfirstlane(need);
    budget = __builtin_amdgcn_readfirstlane(budget);
    const unsigned end = need + 32;
    asm volatile(
        "v_mov_b32 v86, 2\n\tds_read_b32 v88, v87\n\tds_read_b128 v[72:75], v76 offset:0\n\tds_read_b64 v[62:63], v77 offset:0\n\ts_waitcnt lgkmcnt(0)\n\t"
        "v_mfma_f32_32x32x16_bf16 v[18:33], v[72:75], v[50:53], 0\n\tv_mfma_f32_32x32x8_bf16 v[18:33], v[62:63], v[78:79], v[18:33]\n\t"
        "ds_read_b128 v[72:75], v76 offset:1536\n\tds_read_b64 v[62:63], v77 offset:1536\n\ts_movk_i32 s22, 0x3000\n\ts_waitcnt lgkmcnt(0)\n\ts_nop 4\n\t"
        "v_cmp_gt_u32 vcc, %[need], v88\n\ts_cbranch_vccnz 70f\n\t60:\n\ts_waitcnt lgkmcnt(3)\n\tv_mfma_f32_32x32x16_bf16 v[34:49], v[72:75], v[50:53], 0\n\t"
        "ds_read_b64_tr_b16 v[64:65], v84 offset:0\n\tds_read_b64_tr_b16 v[66:67], v84 offset:384\n\tv_exp_f32 v18, v18\n\tv_exp_f32 v19, v19\n\t"
        "v_exp_f32 v20, v20\n\tv_exp_f32 v21, v21\n\tv_exp_f32 v22, v22\n\ts_waitcnt lgkmcnt(2)\n\tv_exp_f32 v23, v23\n\tv_cvt_pk_bf16_f32 v58, v18, v19\n\t"
        "v_exp_f32 v24, v24\n\tv_exp_f32 v25, v25\n\tv_cvt_pk_bf16_f32 v59, v20, v21\n\tv_mfma_f32_32x32x8_bf16 v[34:49], v[62:63], v[78:79], v[34:49]\n\t"
        "ds_read_b128 v[72:75], v76 offset:3072\n\tds_read_b64 v[62:63], v77 offset:3072\n\ts_add_u32 %[need], %[need], 1\n\tv_exp_f32 v26, v26\n\t"
        "v_exp_f32 v27, v27\n\tv_cvt_pk_bf16_f32 v60, v22, v23\n\tv_exp_f32 v28, v28\n\tv_exp_f32 v29, v29\n\tv_cvt_pk_bf16_f32 v61, v24, v25\n\t"
        "v_exp_f32 v80, v30\n\ts_waitcnt lgkmcnt(2)\n\tv_mfma_f32_32x32x16_bf16 v[2:17], v[64:67], v[58:61], 0\n\t"
        "ds_read_b64_tr_b16 v[68:69], v84 offset:768\n\tds_read_b64_tr_b16 v[70:71], v84 offset:1152\n\tv_exp_f32 v81, v31\n\t"
        "v_cvt_pk_bf16_f32 v54, v26, v27\n\tv_exp_f32 v82, v32\n\tv_exp_f32 v83, v33\n\tv_cvt_pk_bf16_f32 v55, v28, v29\n\ts_waitcnt lgkmcnt(3)\n\t"
        "v_mfma_f32_32x32x16_bf16 v[18:33], v[72:75], v[50:53], 0\n\tds_read_b64_tr_b16 v[64:65], v84 offset:1536\n\t"
        "ds_read_b64_tr_b16 v[66:67], v84 offset:1920\n\tv_exp_f32 v34, v34\n\tv_exp_f32 v35, v35\n\tv_cvt_pk_bf16_f32 v56, v80, v81\n\tv_exp_f32 v36, v36\n\t"
        "v_exp_f32 v37, v37\n\tv_cvt_pk_bf16_f32 v57, v82, v83\n\tv_exp_f32 v38, v38\n\ts_waitcnt lgkmcnt(2)\n\t"
        "v_mfma_f32_32x32x16_bf16 v[2:17], v[68:71], v[54:57], v[2:17]\n\tds_read_b32 v88, v87 offset:8\n\tv_exp_f32 v39, v39\n\t"
        "v_cvt_pk_bf16_f32 v58, v34, v35\n\tv_exp_f32 v40, v40\n\tv_exp_f32 v41, v41\n\tv_cvt_pk_bf16_f32 v59, v36, v37\n\t"
        "v_mfma_f32_32x32x8_bf16 v[18:33], v[62:63], v[78:79], v[18:33]\n\tds_read_b128 v[72:75], v76 offset:4608\n\tds_read_b64 v[62:63], v77 offset:4608\n\t"
        "v_exp_f32 v42, v42\n\tv_exp_f32 v43, v43\n\tv_cvt_pk_bf16_f32 v60, v38, v39\n\tv_exp_f32 v44, v44\n\tv_exp_f32 v45, v45\n\t"
        "v_cvt_pk_bf16_f32 v61, v40, v41\n\tv_exp_f32 v80, v46\n\ts_waitcnt lgkmcnt(2)\n\tv_mfma_f32_32x32x16_bf16 v[2:17], v[64:67], v[58:61], v[2:17]\n\t"
        "ds_read_b64_tr_b16 v[68:69], v84 offset:2304\n\tds_read_b64_tr_b16 v[70:71], v84 offset:2688\n\tv_exp_f32 v81, v47\n\t"
        "v_cvt_pk_bf16_f32 v54, v42, v43\n\tv_exp_f32 v82, v48\n\tv_exp_f32 v83, v49\n\tv_cvt_pk_bf16_f32 v55, v44, v45\n\tv_cmp_gt_u32 vcc, %[need], v88\n\t"
        "s_cbranch_vccnz 72f\n\t62:\n\ts_waitcnt lgkmcnt(3)\n\tv_mfma_f32_32x32x16_bf16 v[34:49], v[72:75], v[50:53], 0\n\t"
        "ds_read_b64_tr_b16 v[64:65], v84 offset:3072\n\tds_read_b64_tr_b16 v[66:67], v84 offset:3456\n\tv_exp_f32 v18, v18\n\tv_exp_f32 v19, v19\n\t"
        "v_cvt_pk_bf16_f32 v56, v80, v81\n\tv_exp_f32 v20, v20\n\tv_exp_f32 v21, v21\n\tv_cvt_pk_bf16_f32 v57, v82, v83\n\tv_exp_f32 v22, v22\n\t"
        "s_waitcnt lgkmcnt(2)\n\tv_mfma_f32_32x32x16_bf16 v[2:17], v[68:71], v[54:57], v[2:17]\n\tds_add_u32 v85, v86\n\tv_exp_f32 v23, v23\n\t"
        "v_cvt_pk_bf16_f32 v58, v18, v19\n\tv_exp_f32 v24, v24\n\tv_exp_f32 v25, v25\n\tv_cvt_pk_bf16_f32 v59, v20, v21\n\t"
        "v_mfma_f32_32x32x8_bf16 v[34:49], v[62:63], v[78:79], v[34:49]\n\tds_read_b128 v[72:75], v76 offset:6144\n\tds_read_b64 v[62:63], v77 offset:6144\n\t"
        "s_add_u32 %[need], %[need], 1\n\tv_exp_f32 v26, v26\n\tv_exp_f32 v27, v27\n\tv_cvt_pk_bf16_f32 v60, v22, v23\n\tv_exp_f32 v28, v28\n\t"
        "v_exp_f32 v29, v29\n\tv_cvt_pk_bf16_f32 v61, v24, v25\n\tv_exp_f32 v80, v30\n\ts_waitcnt lgkmcnt(2)\n\t"
        "v_mfma_f32_32x32x16_bf16 v[2:17], v[64:67], v[58:61], v[2:17]\n\tds_read_b64_tr_b16 v[68:69], v84 offset:3840\n\t"
        "ds_read_b64_tr_b16 v[70:71], v84 offset:4224\n\tv_exp_f32 v81, v31\n\tv_cvt_pk_bf16_f32 v54, v26, v27\n\tv_exp_f32 v82, v32\n\tv_exp_f32 v83, v33\n\t"
        "v_cvt_pk_bf16_f32 v55, v28, v29\n\ts_waitcnt lgkmcnt(3)\n\tv_mfma_f32_32x32x16_bf16 v[18:33], v[72:75], v[50:53], 0\n\t"
        "ds_read_b64_tr_b16 v[64:65], v84 offset:4608\n\tds_read_b64_tr_b16 v[66:67], v84 offset:4992\n\tv_exp_f32 v34, v34\n\tv_exp_f32 v35, v35\n\t"
        "v_cvt_pk_bf16_f32 v56, v80, v81\n\tv_exp_f32 v36, v36\n\tv_exp_f32 v37, v37\n\tv_cvt_pk_bf16_f32 v57, v82, v83\n\tv_exp_f32 v38, v38\n\t"
        "s_waitcnt lgkmcnt(2)\n\tv_mfma_f32_32x32x16_bf16 v[2:17], v[68:71], v[54:57], v[2:17]\n\tds_read_b32 v88, v87 offset:16\n\tv_exp_f32 v39, v39\n\t"
        "v_cvt_pk_bf16_f32 v58, v34, v35\n\tv_exp_f32 v40, v40\n\tv_exp_f32 v41, v41\n\tv_cvt_pk_bf16_f32 v59, v36, v37\n\t"
        "v_mfma_f32_32x32x8_bf16 v[18:33], v[62:63], v[78:79], v[18:33]\n\tds_read_b128 v[72:75], v76 offset:7680\n\tds_read_b64 v[62:63], v77 offset:7680\n\t"
        "v_exp_f32 v42, v42\n\tv_exp_f32 v43, v43\n\tv_cvt_pk_bf16_f32 v60, v38, v39\n\tv_exp_f32 v44, v44\n\tv_exp_f32 v45, v45\n\t"
        "v_cvt_pk_bf16_f32 v61, v40, v41\n\tv_exp_f32 v80, v46\n\ts_waitcnt lgkmcnt(2)\n\tv_mfma_f32_32x32x16_bf16 v[2:17], v[64:67], v[58:61], v[2:17]\n\t"
        "ds_read_b64_tr_b16 v[68:69], v84 offset:5376\n\tds_read_b64_tr_b16 v[70:71], v84 offset:5760\n\tv_exp_f32 v81, v47\n\t"
        "v_cvt_pk_bf16_f32 v54, v42, v43\n\tv_exp_f32 v82, v48\n\tv_exp_f32 v83, v49\n\tv_cvt_pk_bf16_f32 v55, v44, v45\n\tv_cmp_gt_u32 vcc, %[need], v88\n\t"
        "s_cbranch_vccnz 74f\n\t64:\n\ts_waitcnt lgkmcnt(3)\n\tv_mfma_f32_32x32x16_bf16 v[34:49], v[72:75], v[50:53], 0\n\t"
        "ds_read_b64_tr_b16 v[64:65], v84 offset:6144\n\tds_read_b64_tr_b16 v[66:67], v84 offset:6528\n\tv_exp_f32 v18, v18\n\tv_exp_f32 v19, v19\n\t"
        "v_cvt_pk_bf16_f32 v56, v80, v81\n\tv_exp_f32 v20, v20\n\tv_exp_f32 v21, v21\n\tv_cvt_pk_bf16_f32 v57, v82, v83\n\tv_exp_f32 v22, v22\n\t"
        "s_waitcnt lgkmcnt(2)\n\tv_mfma_f32_32x32x16_bf16 v[2:17], v[68:71], v[54:57], v[2:17]\n\tv_exp_f32 v23, v23\n\tv_cvt_pk_bf16_f32 v58, v18, v19\n\t"
        "v_exp_f32 v24, v24\n\tv_exp_f32 v25, v25\n\tv_cvt_pk_bf16_f32 v59, v20, v21\n\tv_mfma_f32_32x32x8_bf16 v[34:49], v[62:63], v[78:79], v[34:49]\n\t"
        "ds_read_b128 v[72:75], v76 offset:9216\n\tds_read_b64 v[62:63], v77 offset:9216\n\ts_add_u32 %[need], %[need], 1\n\tv_exp_f32 v26, v26\n\t"
        "v_exp_f32 v27, v27\n\tv_cvt_pk_bf16_f32 v60, v22, v23\n\tv_exp_f32 v28, v28\n\tv_exp_f32 v29, v29\n\tv_cvt_pk_bf16_f32 v61, v24, v25\n\t"
        "v_exp_f32 v80, v30\n\ts_waitcnt lgkmcnt(2)\n\tv_mfma_f32_32x32x16_bf16 v[2:17], v[64:67], v[58:61], v[2:17]\n\t"
        "ds_read_b64_tr_b16 v[68:69], v84 offset:6912\n\tds_read_b64_tr_b16 v[70:71], v84 offset:7296\n\tv_exp_f32 v81, v31\n\t"
        "v_cvt_pk_bf16_f32 v54, v26, v27\n\tv_exp_f32 v82, v32\n\tv_exp_f32 v83, v33\n\tv_cvt_pk_bf16_f32 v55, v28, v29\n\ts_waitcnt lgkmcnt(3)\n\t"
        "v_mfma_f32_32x32x16_bf16 v[18:33], v[72:75], v[50:53], 0\n\tds_read_b64_tr_b16 v[64:65], v84 offset:7680\n\t"
        "ds_read_b64_tr_b16 v[66:67], v84 offset:8064\n\tv_exp_f32 v34, v34\n\tv_exp_f32 v35, v35\n\tv_cvt_pk_bf16_f32 v56, v80, v81\n\tv_exp_f32 v36, v36\n\t"
        "v_exp_f32 v37, v37\n\tv_cvt_pk_bf16_f32 v57, v82, v83\n\tv_exp_f32 v38, v38\n\ts_waitcnt lgkmcnt(2)\n\t"
        "v_mfma_f32_32x32x16_bf16 v[2:17], v[68:71], v[54:57], v[2:17]\n\tds_read_b32 v88, v87 offset:24\n\tv_exp_f32 v39, v39\n\t"
        "v_cvt_pk_bf16_f32 v58, v34, v35\n\tv_exp_f32 v40, v40\n\tv_exp_f32 v41, v41\n\tv_cvt_pk_bf16_f32 v59, v36, v37\n\t"
        "v_mfma_f32_32x32x8_bf16 v[18:33], v[62:63], v[78:79], v[18:33]\n\tds_read_b128 v[72:75], v76 offset:10752\n\t"
        "ds_read_b64 v[62:63], v77 offset:10752\n\tv_exp_f32 v42, v42\n\tv_exp_f32 v43, v43\n\tv_cvt_pk_bf16_f32 v60, v38, v39\n\tv_exp_f32 v44, v44\n\t"
        "v_exp_f32 v45, v45\n\tv_cvt_pk_bf16_f32 v61, v40, v41\n\tv_exp_f32 v80, v46\n\ts_waitcnt lgkmcnt(2)\n\t"
        "v_mfma_f32_32x32x16_bf16 v[2:17], v[64:67], v[58:61], v[2:17]\n\tds_read_b64_tr_b16 v[68:69], v84 offset:8448\n\t"
        "ds_read_b64_tr_b16 v[70:71], v84 offset:8832\n\tv_exp_f32 v81, v47\n\tv_cvt_pk_bf16_f32 v54, v42, v43\n\tv_exp_f32 v82, v48\n\tv_exp_f32 v83, v49\n\t"
        "v_cvt_pk_bf16_f32 v55, v44, v45\n\tv_cmp_gt_u32 vcc, %[need], v88\n\ts_cbranch_vccnz 76f\n\t66:\n\ts_waitcnt lgkmcnt(3)\n\t"
        "v_mfma_f32_32x32x16_bf16 v[34:49], v[72:75], v[50:53], 0\n\tds_read_b64_tr_b16 v[64:65], v84 offset:9216\n\t"
        "ds_read_b64_tr_b16 v[66:67], v84 offset:9600\n\tv_exp_f32 v18, v18\n\tv_exp_f32 v19, v19\n\tv_cvt_pk_bf16_f32 v56, v80, v81\n\tv_exp_f32 v20, v20\n\t"
        "v_exp_f32 v21, v21\n\tv_cvt_pk_bf16_f32 v57, v82, v83\n\tv_exp_f32 v22, v22\n\ts_waitcnt lgkmcnt(2)\n\t"
        "v_mfma_f32_32x32x16_bf16 v[2:17], v[68:71], v[54:57], v[2:17]\n\tds_add_u32 v85, v86\n\tv_exp_f32 v23, v23\n\tv_cvt_pk_bf16_f32 v58, v18, v19\n\t"
        "v_exp_f32 v24, v24\n\tv_exp_f32 v25, v25\n\tv_cvt_pk_bf16_f32 v59, v20, v21\n\tv_add_u32 v76, s22, v76\n\tv_add_u32 v77, s22, v77\n\t"
        "v_mov_b32 v87, %[rdyb]\n\tv_mfma_f32_32x32x8_bf16 v[34:49], v[62:63], v[78:79], v[34:49]\n\tds_read_b128 v[72:75], v76 offset:0\n\t"
        "ds_read_b64 v[62:63], v77 offset:0\n\ts_add_u32 %[need], %[need], 2\n\tv_exp_f32 v26, v26\n\tv_exp_f32 v27, v27\n\tv_cvt_pk_bf16_f32 v60, v22, v23\n\t"
        "v_exp_f32 v28, v28\n\tv_exp_f32 v29, v29\n\tv_cvt_pk_bf16_f32 v61, v24, v25\n\tv_exp_f32 v80, v30\n\ts_waitcnt lgkmcnt(2)\n\t"
        "v_mfma_f32_32x32x16_bf16 v[2:17], v[64:67], v[58:61], v[2:17]\n\tds_read_b64_tr_b16 v[68:69], v84 offset:9984\n\t"
        "ds_read_b64_tr_b16 v[70:71], v84 offset:10368\n\tv_exp_f32 v81, v31\n\tv_cvt_pk_bf16_f32 v54, v26, v27\n\tv_exp_f32 v82, v32\n\tv_exp_f32 v83, v33\n\t"
        "v_cvt_pk_bf16_f32 v55, v28, v29\n\ts_waitcnt lgkmcnt(3)\n\tv_mfma_f32_32x32x16_bf16 v[18:33], v[72:75], v[50:53], 0\n\t"
        "ds_read_b64_tr_b16 v[64:65], v84 offset:10752\n\tds_read_b64_tr_b16 v[66:67], v84 offset:11136\n\tv_exp_f32 v34, v34\n\tv_exp_f32 v35, v35\n\t"
        "v_cvt_pk_bf16_f32 v56, v80, v81\n\tv_exp_f32 v36, v36\n\tv_exp_f32 v37, v37\n\tv_cvt_pk_bf16_f32 v57, v82, v83\n\tv_exp_f32 v38, v38\n\t"
        "s_waitcnt lgkmcnt(2)\n\tv_mfma_f32_32x32x16_bf16 v[2:17], v[68:71], v[54:57], v[2:17]\n\tds_read_b32 v88, v87 offset:0\n\tv_exp_f32 v39, v39\n\t"
        "v_cvt_pk_bf16_f32 v58, v34, v35\n\tv_exp_f32 v40, v40\n\tv_exp_f32 v41, v41\n\tv_cvt_pk_bf16_f32 v59, v36, v37\n\t"
        "v_mfma_f32_32x32x8_bf16 v[18:33], v[62:63], v[78:79], v[18:33]\n\tds_read_b128 v[72:75], v76 offset:1536\n\tds_read_b64 v[62:63], v77 offset:1536\n\t"
        "v_exp_f32 v42, v42\n\tv_exp_f32 v43, v43\n\tv_cvt_pk_bf16_f32 v60, v38, v39\n\tv_exp_f32 v44, v44\n\tv_exp_f32 v45, v45\n\t"
        "v_cvt_pk_bf16_f32 v61, v40, v41\n\tv_exp_f32 v80, v46\n\ts_waitcnt lgkmcnt(2)\n\tv_mfma_f32_32x32x16_bf16 v[2:17], v[64:67], v[58:61], v[2:17]\n\t"
        "ds_read_b64_tr_b16 v[68:69], v84 offset:11520\n\tds_read_b64_tr_b16 v[70:71], v84 offset:11904\n\tv_exp_f32 v81, v47\n\tv_add_u32 v84, s22, v84\n\t"
        "s_sub_u32 s22, 0, s22\n\ts_cmp_lt_u32 %[need], %[end]\n\tv_cvt_pk_bf16_f32 v54, v42, v43\n\tv_exp_f32 v82, v48\n\tv_exp_f32 v83, v49\n\t"
        "v_cvt_pk_bf16_f32 v55, v44, v45\n\t.p2align 6\n\t3:\n\ts_setprio 3\n\tv_cmp_gt_u32 vcc, %[need], v88\n\ts_cbranch_vccnz 20f\n\t10:\n\t"
        "s_waitcnt lgkmcnt(3)\n\tv_mfma_f32_32x32x16_bf16 v[34:49], v[72:75], v[50:53], 0\n\tds_read_b64_tr_b16 v[64:65], v84 offset:0\n\t"
        "ds_read_b64_tr_b16 v[66:67], v84 offset:384\n\tv_exp_f32 v18, v18\n\tv_exp_f32 v19, v19\n\tv_cvt_pk_bf16_f32 v56, v80, v81\n\tv_exp_f32 v20, v20\n\t"
        "v_exp_f32 v21, v21\n\tv_cvt_pk_bf16_f32 v57, v82, v83\n\tv_exp_f32 v22, v22\n\ts_waitcnt lgkmcnt(2)\n\t"
        "v_mfma_f32_32x32x16_bf16 v[2:17], v[68:71], v[54:57], v[2:17]\n\tv_exp_f32 v23, v23\n\tv_cvt_pk_bf16_f32 v58, v18, v19\n\tv_exp_f32 v24, v24\n\t"
        "v_exp_f32 v25, v25\n\tv_cvt_pk_bf16_f32 v59, v20, v21\n\tv_mfma_f32_32x32x8_bf16 v[34:49], v[62:63], v[78:79], v[34:49]\n\t"
        "ds_read_b128 v[72:75], v76 offset:3072\n\tds_read_b64 v[62:63], v77 offset:3072\n\ts_add_u32 %[need], %[need], 2\n\tv_exp_f32 v26, v26\n\t"
        "v_exp_f32 v27, v27\n\tv_cvt_pk_bf16_f32 v60, v22, v23\n\tv_exp_f32 v28, v28\n\tv_exp_f32 v29, v29\n\tv_cvt_pk_bf16_f32 v61, v24, v25\n\t"
        "v_exp_f32 v80, v30\n\ts_waitcnt lgkmcnt(2)\n\tv_mfma_f32_32x32x16_bf16 v[2:17], v[64:67], v[58:61], v[2:17]\n\t"
        "ds_read_b64_tr_b16 v[68:69], v84 offset:768\n\tds_read_b64_tr_b16 v[70:71], v84 offset:1152\n\tv_exp_f32 v81, v31\n\t"
        "v_cvt_pk_bf16_f32 v54, v26, v27\n\tv_exp_f32 v82, v32\n\tv_exp_f32 v83, v33\n\tv_cvt_pk_bf16_f32 v55, v28, v29\n\ts_setprio 2\n\t"
        "s_waitcnt lgkmcnt(3)\n\tv_mfma_f32_32x32x16_bf16 v[18:33], v[72:75], v[50:53], 0\n\tds_read_b64_tr_b16 v[64:65], v84 offset:1536\n\t"
        "ds_read_b64_tr_b16 v[66:67], v84 offset:1920\n\tv_exp_f32 v34, v34\n\tv_exp_f32 v35, v35\n\tv_cvt_pk_bf16_f32 v56, v80, v81\n\tv_exp_f32 v36, v36\n\t"
        "v_exp_f32 v37, v37\n\tv_cvt_pk_bf16_f32 v57, v82, v83\n\tv_exp_f32 v38, v38\n\ts_waitcnt lgkmcnt(2)\n\t"
        "v_mfma_f32_32x32x16_bf16 v[2:17], v[68:71], v[54:57], v[2:17]\n\tv_exp_f32 v39, v39\n\tv_cvt_pk_bf16_f32 v58, v34, v35\n\tv_exp_f32 v40, v40\n\t"
        "v_exp_f32 v41, v41\n\tv_cvt_pk_bf16_f32 v59, v36, v37\n\tv_mfma_f32_32x32x8_bf16 v[18:33], v[62:63], v[78:79], v[18:33]\n\t"
        "ds_read_b128 v[72:75], v76 offset:4608\n\tds_read_b64 v[62:63], v77 offset:4608\n\tv_exp_f32 v42, v42\n\tv_exp_f32 v43, v43\n\t"
        "v_cvt_pk_bf16_f32 v60, v38, v39\n\tv_exp_f32 v44, v44\n\tv_exp_f32 v45, v45\n\tv_cvt_pk_bf16_f32 v61, v40, v41\n\tv_exp_f32 v80, v46\n\t"
        "s_waitcnt lgkmcnt(2)\n\tv_mfma_f32_32x32x16_bf16 v[2:17], v[64:67], v[58:61], v[2:17]\n\tds_read_b64_tr_b16 v[68:69], v84 offset:2304\n\t"
        "ds_read_b64_tr_b16 v[70:71], v84 offset:2688\n\tv_exp_f32 v81, v47\n\tv_cvt_pk_bf16_f32 v54, v42, v43\n\tv_exp_f32 v82, v48\n\tv_exp_f32 v83, v49\n\t"
        "v_cvt_pk_bf16_f32 v55, v44, v45\n\ts_setprio 1\n\ts_waitcnt lgkmcnt(3)\n\tv_mfma_f32_32x32x16_bf16 v[34:49], v[72:75], v[50:53], 0\n\t"
        "ds_read_b64_tr_b16 v[64:65], v84 offset:3072\n\tds_read_b64_tr_b16 v[66:67], v84 offset:3456\n\tv_exp_f32 v18, v18\n\tv_exp_f32 v19, v19\n\t"
        "v_cvt_pk_bf16_f32 v56, v80, v81\n\tv_exp_f32 v20, v20\n\tv_exp_f32 v21, v21\n\tv_cvt_pk_bf16_f32 v57, v82, v83\n\tv_exp_f32 v22, v22\n\t"
        "s_waitcnt lgkmcnt(2)\n\tv_mfma_f32_32x32x16_bf16 v[2:17], v[68:71], v[54:57], v[2:17]\n\tds_add_u32 v85, v86\n\tv_exp_f32 v23, v23\n\t"
        "v_cvt_pk_bf16_f32 v58, v18, v19\n\tv_exp_f32 v24, v24\n\tv_exp_f32 v25, v25\n\tv_cvt_pk_bf16_f32 v59, v20, v21\n\t"
        "v_mfma_f32_32x32x8_bf16 v[34:49], v[62:63], v[78:79], v[34:49]\n\tds_read_b128 v[72:75], v76 offset:6144\n\tds_read_b64 v[62:63], v77 offset:6144\n\t"
        "v_exp_f32 v26, v26\n\tv_exp_f32 v27, v27\n\tv_cvt_pk_bf16_f32 v60, v22, v23\n\tv_exp_f32 v28, v28\n\tv_exp_f32 v29, v29\n\t"
        "v_cvt_pk_bf16_f32 v61, v24, v25\n\tv_exp_f32 v80, v30\n\ts_waitcnt lgkmcnt(2)\n\tv_mfma_f32_32x32x16_bf16 v[2:17], v[64:67], v[58:61], v[2:17]\n\t"
        "ds_read_b64_tr_b16 v[68:69], v84 offset:3840\n\tds_read_b64_tr_b16 v[70:71], v84 offset:4224\n\tv_exp_f32 v81, v31\n\t"
        "v_cvt_pk_bf16_f32 v54, v26, v27\n\tv_exp_f32 v82, v32\n\tv_exp_f32 v83, v33\n\tv_cvt_pk_bf16_f32 v55, v28, v29\n\ts_setprio 0\n\t"
        "s_waitcnt lgkmcnt(3)\n\tv_mfma_f32_32x32x16_bf16 v[18:33], v[72:75], v[50:53], 0\n\tds_read_b64_tr_b16 v[64:65], v84 offset:4608\n\t"
        "ds_read_b64_tr_b16 v[66:67], v84 offset:4992\n\tv_exp_f32 v34, v34\n\tv_exp_f32 v35, v35\n\tv_cvt_pk_bf16_f32 v56, v80, v81\n\tv_exp_f32 v36, v36\n\t"
        "v_exp_f32 v37, v37\n\tv_cvt_pk_bf16_f32 v57, v82, v83\n\tv_exp_f32 v38, v38\n\ts_waitcnt lgkmcnt(2)\n\t"
        "v_mfma_f32_32x32x16_bf16 v[2:17], v[68:71], v[54:57], v[2:17]\n\tds_read_b32 v88, v87 offset:16\n\tv_exp_f32 v39, v39\n\t"
        "v_cvt_pk_bf16_f32 v58, v34, v35\n\tv_exp_f32 v40, v40\n\tv_exp_f32 v41, v41\n\tv_cvt_pk_bf16_f32 v59, v36, v37\n\t"
        "v_mfma_f32_32x32x8_bf16 v[18:33], v[62:63], v[78:79], v[18:33]\n\tds_read_b128 v[72:75], v76 offset:7680\n\tds_read_b64 v[62:63], v77 offset:7680\n\t"
        "v_exp_f32 v42, v42\n\tv_exp_f32 v43, v43\n\tv_cvt_pk_bf16_f32 v60, v38, v39\n\tv_exp_f32 v44, v44\n\tv_exp_f32 v45, v45\n\t"
        "v_cvt_pk_bf16_f32 v61, v40, v41\n\tv_exp_f32 v80, v46\n\ts_waitcnt lgkmcnt(2)\n\tv_mfma_f32_32x32x16_bf16 v[2:17], v[64:67], v[58:61], v[2:17]\n\t"
        "ds_read_b64_tr_b16 v[68:69], v84 offset:5376\n\tds_read_b64_tr_b16 v[70:71], v84 offset:5760\n\tv_exp_f32 v81, v47\n\t"
        "v_cvt_pk_bf16_f32 v54, v42, v43\n\tv_exp_f32 v82, v48\n\tv_exp_f32 v83, v49\n\tv_cvt_pk_bf16_f32 v55, v44, v45\n\ts_setprio 3\n\t"
        "v_cmp_gt_u32 vcc, %[need], v88\n\ts_cbranch_vccnz 24f\n\t14:\n\ts_waitcnt lgkmcnt(3)\n\tv_mfma_f32_32x32x16_bf16 v[34:49], v[72:75], v[50:53], 0\n\t"
        "ds_read_b64_tr_b16 v[64:65], v84 offset:6144\n\tds_read_b64_tr_b16 v[66:67], v84 offset:6528\n\tv_exp_f32 v18, v18\n\tv_exp_f32 v19, v19\n\t"
        "v_cvt_pk_bf16_f32 v56, v80, v81\n\tv_exp_f32 v20, v20\n\tv_exp_f32 v21, v21\n\tv_cvt_pk_bf16_f32 v57, v82, v83\n\tv_exp_f32 v22, v22\n\t"
        "s_waitcnt lgkmcnt(2)\n\tv_mfma_f32_32x32x16_bf16 v[2:17], v[68:71], v[54:57], v[2:17]\n\tv_exp_f32 v23, v23\n\tv_cvt_pk_bf16_f32 v58, v18, v19\n\t"
        "v_exp_f32 v24, v24\n\tv_exp_f32 v25, v25\n\tv_cvt_pk_bf16_f32 v59, v20, v21\n\tv_mfma_f32_32x32x8_bf16 v[34:49], v[62:63], v[78:79], v[34:49]\n\t"
        "ds_read_b128 v[72:75], v76 offset:9216\n\tds_read_b64 v[62:63], v77 offset:9216\n\ts_add_u32 %[need], %[need], 2\n\tv_exp_f32 v26, v26\n\t"
        "v_exp_f32 v27, v27\n\tv_cvt_pk_bf16_f32 v60, v22, v23\n\tv_exp_f32 v28, v28\n\tv_exp_f32 v29, v29\n\tv_cvt_pk_bf16_f32 v61, v24, v25\n\t"
        "v_exp_f32 v80, v30\n\ts_waitcnt lgkmcnt(2)\n\tv_mfma_f32_32x32x16_bf16 v[2:17], v[64:67], v[58:61], v[2:17]\n\t"
        "ds_read_b64_tr_b16 v[68:69], v84 offset:6912\n\tds_read_b64_tr_b16 v[70:71], v84 offset:7296\n\tv_exp_f32 v81, v31\n\t"
        "v_cvt_pk_bf16_f32 v54, v26, v27\n\tv_exp_f32 v82, v32\n\tv_exp_f32 v83, v33\n\tv_cvt_pk_bf16_f32 v55, v28, v29\n\ts_setprio 2\n\t"
        "s_waitcnt lgkmcnt(3)\n\tv_mfma_f32_32x32x16_bf16 v[18:33], v[72:75], v[50:53], 0\n\tds_read_b64_tr_b16 v[64:65], v84 offset:7680\n\t"
        "ds_read_b64_tr_b16 v[66:67], v84 offset:8064\n\tv_exp_f32 v34, v34\n\tv_exp_f32 v35, v35\n\tv_cvt_pk_bf16_f32 v56, v80, v81\n\tv_exp_f32 v36, v36\n\t"
        "v_exp_f32 v37, v37\n\tv_cvt_pk_bf16_f32 v57, v82, v83\n\tv_exp_f32 v38, v38\n\ts_waitcnt lgkmcnt(2)\n\t"
        "v_mfma_f32_32x32x16_bf16 v[2:17], v[68:71], v[54:57], v[2:17]\n\tv_exp_f32 v39, v39\n\tv_cvt_pk_bf16_f32 v58, v34, v35\n\tv_exp_f32 v40, v40\n\t"
        "v_exp_f32 v41, v41\n\tv_cvt_pk_bf16_f32 v59, v36, v37\n\tv_mfma_f32_32x32x8_bf16 v[18:33], v[62:63], v[78:79], v[18:33]\n\t"
        "ds_read_b128 v[72:75], v76 offset:10752\n\tds_read_b64 v[62:63], v77 offset:10752\n\tv_exp_f32 v42, v42\n\tv_exp_f32 v43, v43\n\t"
        "v_cvt_pk_bf16_f32 v60, v38, v39\n\tv_exp_f32 v44, v44\n\tv_exp_f32 v45, v45\n\tv_cvt_pk_bf16_f32 v61, v40, v41\n\tv_exp_f32 v80, v46\n\t"
        "s_waitcnt lgkmcnt(2)\n\tv_mfma_f32_32x32x16_bf16 v[2:17], v[64:67], v[58:61], v[2:17]\n\tds_read_b64_tr_b16 v[68:69], v84 offset:8448\n\t"
        "ds_read_b64_tr_b16 v[70:71], v84 offset:8832\n\tv_exp_f32 v81, v47\n\tv_cvt_pk_bf16_f32 v54, v42, v43\n\tv_exp_f32 v82, v48\n\tv_exp_f32 v83, v49\n\t"
        "v_cvt_pk_bf16_f32 v55, v44, v45\n\ts_setprio 1\n\ts_waitcnt lgkmcnt(3)\n\tv_mfma_f32_32x32x16_bf16 v[34:49], v[72:75], v[50:53], 0\n\t"
        "ds_read_b64_tr_b16 v[64:65], v84 offset:9216\n\tds_read_b64_tr_b16 v[66:67], v84 offset:9600\n\tv_exp_f32 v18, v18\n\tv_exp_f32 v19, v19\n\t"
        "v_cvt_pk_bf16_f32 v56, v80, v81\n\tv_exp_f32 v20, v20\n\tv_exp_f32 v21, v21\n\tv_cvt_pk_bf16_f32 v57, v82, v83\n\tv_exp_f32 v22, v22\n\t"
        "s_waitcnt lgkmcnt(2)\n\tv_mfma_f32_32x32x16_bf16 v[2:17], v[68:71], v[54:57], v[2:17]\n\tds_add_u32 v85, v86\n\tv_exp_f32 v23, v23\n\t"
        "v_cvt_pk_bf16_f32 v58, v18, v19\n\tv_exp_f32 v24, v24\n\tv_exp_f32 v25, v25\n\tv_cvt_pk_bf16_f32 v59, v20, v21\n\tv_add_u32 v76, s22, v76\n\t"
        "v_add_u32 v77, s22, v77\n\tv_mfma_f32_32x32x8_bf16 v[34:49], v[62:63], v[78:79], v[34:49]\n\tds_read_b128 v[72:75], v76 offset:0\n\t"
        "ds_read_b64 v[62:63], v77 offset:0\n\tv_exp_f32 v26, v26\n\tv_exp_f32 v27, v27\n\tv_cvt_pk_bf16_f32 v60, v22, v23\n\tv_exp_f32 v28, v28\n\t"
        "v_exp_f32 v29, v29\n\tv_cvt_pk_bf16_f32 v61, v24, v25\n\tv_exp_f32 v80, v30\n\ts_waitcnt lgkmcnt(2)\n\t"
        "v_mfma_f32_32x32x16_bf16 v[2:17], v[64:67], v[58:61], v[2:17]\n\tds_read_b64_tr_b16 v[68:69], v84 offset:9984\n\t"
        "ds_read_b64_tr_b16 v[70:71], v84 offset:10368\n\tv_exp_f32 v81, v31\n\tv_cvt_pk_bf16_f32 v54, v26, v27\n\tv_exp_f32 v82, v32\n\tv_exp_f32 v83, v33\n\t"
        "v_cvt_pk_bf16_f32 v55, v28, v29\n\ts_setprio 0\n\ts_waitcnt lgkmcnt(3)\n\tv_mfma_f32_32x32x16_bf16 v[18:33], v[72:75], v[50:53], 0\n\t"
        "ds_read_b64_tr_b16 v[64:65], v84 offset:10752\n\tds_read_b64_tr_b16 v[66:67], v84 offset:11136\n\tv_exp_f32 v34, v34\n\tv_exp_f32 v35, v35\n\t"
        "v_cvt_pk_bf16_f32 v56, v80, v81\n\tv_exp_f32 v36, v36\n\tv_exp_f32 v37, v37\n\tv_cvt_pk_bf16_f32 v57, v82, v83\n\tv_exp_f32 v38, v38\n\t"
        "s_waitcnt lgkmcnt(2)\n\tv_mfma_f32_32x32x16_bf16 v[2:17], v[68:71], v[54:57], v[2:17]\n\tds_read_b32 v88, v87 offset:0\n\tv_exp_f32 v39, v39\n\t"
        "v_cvt_pk_bf16_f32 v58, v34, v35\n\tv_exp_f32 v40, v40\n\tv_exp_f32 v41, v41\n\tv_cvt_pk_bf16_f32 v59, v36, v37\n\t"
        "v_mfma_f32_32x32x8_bf16 v[18:33], v[62:63], v[78:79], v[18:33]\n\tds_read_b128 v[72:75], v76 offset:1536\n\tds_read_b64 v[62:63], v77 offset:1536\n\t"
        "v_exp_f32 v42, v42\n\tv_exp_f32 v43, v43\n\tv_cvt_pk_bf16_f32 v60, v38, v39\n\tv_exp_f32 v44, v44\n\tv_exp_f32 v45, v45\n\t"
        "v_cvt_pk_bf16_f32 v61, v40, v41\n\tv_exp_f32 v80, v46\n\ts_waitcnt lgkmcnt(2)\n\tv_mfma_f32_32x32x16_bf16 v[2:17], v[64:67], v[58:61], v[2:17]\n\t"
        "ds_read_b64_tr_b16 v[68:69], v84 offset:11520\n\tds_read_b64_tr_b16 v[70:71], v84 offset:11904\n\tv_exp_f32 v81, v47\n\tv_add_u32 v84, s22, v84\n\t"
        "s_sub_u32 s22, 0, s22\n\ts_cmp_lt_u32 %[need], %[end]\n\tv_cvt_pk_bf16_f32 v54, v42, v43\n\tv_exp_f32 v82, v48\n\tv_exp_f32 v83, v49\n\t"
        "v_cvt_pk_bf16_f32 v55, v44, v45\n\ts_cbranch_scc1 3b\n\ts_branch 4f\n\t20:\n\ts_cmp_eq_u32 %[bud], 0\n\ts_cbranch_scc1 10b\n\t"
        "s_sub_u32 %[bud], %[bud], 1\n\ts_sleep 1\n\tds_read_b32 v88, v87 offset:0\n\ts_waitcnt lgkmcnt(0)\n\tv_cmp_gt_u32 vcc, %[need], v88\n\t"
        "s_cbranch_vccnz 20b\n\ts_branch 10b\n\t24:\n\ts_cmp_eq_u32 %[bud], 0\n\ts_cbranch_scc1 14b\n\ts_sub_u32 %[bud], %[bud], 1\n\ts_sleep 1\n\t"
        "ds_read_b32 v88, v87 offset:16\n\ts_waitcnt lgkmcnt(0)\n\tv_cmp_gt_u32 vcc, %[need], v88\n\ts_cbranch_vccnz 24b\n\ts_branch 14b\n\t70:\n\t"
        "s_cmp_eq_u32 %[bud], 0\n\ts_cbranch_scc1 60b\n\ts_sub_u32 %[bud], %[bud], 1\n\ts_sleep 1\n\tds_read_b32 v88, v87 offset:0\n\ts_waitcnt lgkmcnt(0)\n\t"
        "v_cmp_gt_u32 vcc, %[need], v88\n\ts_cbranch_vccnz 70b\n\ts_branch 60b\n\t72:\n\ts_cmp_eq_u32 %[bud], 0\n\ts_cbranch_scc1 62b\n\t"
        "s_sub_u32 %[bud], %[bud], 1\n\ts_sleep 1\n\tds_read_b32 v88, v87 offset:8\n\ts_waitcnt lgkmcnt(0)\n\tv_cmp_gt_u32 vcc, %[need], v88\n\t"
        "s_cbranch_vccnz 72b\n\ts_branch 62b\n\t74:\n\ts_cmp_eq_u32 %[bud], 0\n\ts_cbranch_scc1 64b\n\ts_sub_u32 %[bud], %[bud], 1\n\ts_sleep 1\n\t"
        "ds_read_b32 v88, v87 offset:16\n\ts_waitcnt lgkmcnt(0)\n\tv_cmp_gt_u32 vcc, %[need], v88\n\ts_cbranch_vccnz 74b\n\ts_branch 64b\n\t76:\n\t"
        "s_cmp_eq_u32 %[bud], 0\n\ts_cbranch_scc1 66b\n\ts_sub_u32 %[bud], %[bud], 1\n\ts_sleep 1\n\tds_read_b32 v88, v87 offset:24\n\ts_waitcnt lgkmcnt(0)\n\t"
        "v_cmp_gt_u32 vcc, %[need], v88\n\ts_cbranch_vccnz 76b\n\ts_branch 66b\n\t4:\n\tv_cvt_pk_bf16_f32 v56, v80, v81\n\tv_cvt_pk_bf16_f32 v57, v82, v83\n\t"
        "s_waitcnt lgkmcnt(0)\n\ts_nop 1\n\tv_mfma_f32_32x32x16_bf16 v[2:17], v[68:71], v[54:57], v[2:17]\n\ts_setprio 0\n\ts_nop 10\n\t"
        : "=&{v[2:17]}"(o), "+{v76}"(ka), "+{v77}"(kta), "+{v84}"(va), "+{v87}"(rdya), [need] "+&s"(need), [bud] "+&s"(budget)
        : "{v[50:53]}"(qm), "{v[78:79]}"(qtl), "{v85}"(proga), [end] "s"(end), [rdyb] "v"(rdyb)
        : "v18", "v19", "v20", "v21", "v22", "v23", "v24", "v25", "v26", "v27", "v28", "v29", "v30", "v31", "v32", "v33", "v34", "v35", "v36", "v37",
          "v38", "v39", "v40", "v41", "v42", "v43", "v44", "v45", "v46", "v47", "v48", "v49", "v54", "v55", "v56", "v57", "v58", "v59", "v60", "v61",
          "v62", "v63", "v64", "v65", "v66", "v67", "v68", "v69", "v70", "v71", "v72", "v73", "v74", "v75", "v80", "v81", "v82", "v83", "v86", "v88",
          "s22", "vcc", "scc", "memory");
    return o;
}

DI f32x16 attn_win(f32x16 s0, s16x8 qm, s16x4 qtl, f32x16 mask, unsigned ka, unsigned kta, unsigned va, unsigned rdya, unsigned need,
                   unsigned pexit, unsigned& budget) {
    f32x16 o;
    need = __builtin_amdgcn_readfirstlane(need);
    budget = __builtin_amdgcn_readfirstlane(budget);
    asm volatile(
        "ds_read_b32 v88, v87 offset:28\n\ts_waitcnt lgkmcnt(0)\n\tv_cmp_gt_u32 vcc, %[need], v88\n\ts_cbranch_vccnz 27f\n\t17:\n\t"
        "ds_read_b128 v[72:75], v76 offset:10752\n\tds_read_b64 v[62:63], v77 offset:10752\n\tds_read_b32 v88, v87 offset:24\n\ts_waitcnt lgkmcnt(0)\n\t"
        "v_mfma_f32_32x32x16_bf16 v[34:49], v[72:75], v[50:53], 0\n\tds_read_b64_tr_b16 v[64:65], v84 offset:12288\n\t"
        "ds_read_b64_tr_b16 v[66:67], v84 offset:12672\n\tv_exp_f32 v18, v18\n\tv_exp_f32 v19, v19\n\tv_exp_f32 v20, v20\n\tv_exp_f32 v21, v21\n\t"
        "v_exp_f32 v22, v22\n\ts_waitcnt lgkmcnt(2)\n\tv_exp_f32 v23, v23\n\tv_cvt_pk_bf16_f32 v58, v18, v19\n\tv_exp_f32 v24, v24\n\tv_exp_f32 v25, v25\n\t"
        "v_cvt_pk_bf16_f32 v59, v20, v21\n\tv_cmp_gt_u32 vcc, %[need], v88\n\ts_cbranch_vccnz 26f\n\t16:\n\t"
        "v_mfma_f32_32x32x8_bf16 v[34:49], v[62:63], v[78:79], v[34:49]\n\tds_read_b128 v[72:75], v76 offset:9216\n\tds_read_b64 v[62:63], v77 offset:9216\n\t"
        "s_cmp_eq_u32 %[pexit], 0\n\tv_exp_f32 v26, v26\n\tv_exp_f32 v27, v27\n\tv_cvt_pk_bf16_f32 v60, v22, v23\n\tv_exp_f32 v28, v28\n\t"
        "v_exp_f32 v29, v29\n\tv_cvt_pk_bf16_f32 v61, v24, v25\n\tv_exp_f32 v80, v30\n\ts_waitcnt lgkmcnt(2)\n\t"
        "v_mfma_f32_32x32x16_bf16 v[2:17], v[64:67], v[58:61], 0\n\tds_read_b32 v88, v87 offset:20\n\tds_read_b64_tr_b16 v[68:69], v84 offset:13056\n\t"
        "ds_read_b64_tr_b16 v[70:71], v84 offset:13440\n\tv_exp_f32 v81, v31\n\tv_cvt_pk_bf16_f32 v54, v26, v27\n\tv_exp_f32 v82, v32\n\tv_exp_f32 v83, v33\n\t"
        "v_cvt_pk_bf16_f32 v55, v28, v29\n\ts_cbranch_scc1 4f\n\ts_waitcnt lgkmcnt(3)\n\tv_mfma_f32_32x32x16_bf16 v[18:33], v[72:75], v[50:53], 0\n\t"
        "ds_read_b64_tr_b16 v[64:65], v84 offset:10752\n\tds_read_b64_tr_b16 v[66:67], v84 offset:11136\n\tv_exp_f32 v34, v34\n\tv_exp_f32 v35, v35\n\t"
        "v_cvt_pk_bf16_f32 v56, v80, v81\n\tv_exp_f32 v36, v36\n\tv_exp_f32 v37, v37\n\tv_cvt_pk_bf16_f32 v57, v82, v83\n\tv_exp_f32 v38, v38\n\t"
        "s_waitcnt lgkmcnt(2)\n\tv_mfma_f32_32x32x16_bf16 v[2:17], v[68:71], v[54:57], v[2:17]\n\tv_exp_f32 v39, v39\n\tv_cvt_pk_bf16_f32 v58, v34, v35\n\t"
        "v_exp_f32 v40, v40\n\tv_exp_f32 v41, v41\n\tv_cvt_pk_bf16_f32 v59, v36, v37\n\tv_cmp_gt_u32 vcc, %[need], v88\n\ts_cbranch_vccnz 25f\n\t15:\n\t"
        "v_mfma_f32_32x32x8_bf16 v[18:33], v[62:63], v[78:79], v[18:33]\n\tds_read_b128 v[72:75], v76 offset:7680\n\tds_read_b64 v[62:63], v77 offset:7680\n\t"
        "s_cmp_eq_u32 %[pexit], 1\n\tv_exp_f32 v42, v42\n\tv_exp_f32 v43, v43\n\tv_cvt_pk_bf16_f32 v60, v38, v39\n\tv_exp_f32 v44, v44\n\t"
        "v_exp_f32 v45, v45\n\tv_cvt_pk_bf16_f32 v61, v40, v41\n\tv_exp_f32 v80, v46\n\ts_waitcnt lgkmcnt(2)\n\t"
        "v_mfma_f32_32x32x16_bf16 v[2:17], v[64:67], v[58:61], v[2:17]\n\tds_read_b32 v88, v87 offset:16\n\tds_read_b64_tr_b16 v[68:69], v84 offset:11520\n\t"
        "ds_read_b64_tr_b16 v[70:71], v84 offset:11904\n\tv_exp_f32 v81, v47\n\tv_cvt_pk_bf16_f32 v54, v42, v43\n\tv_exp_f32 v82, v48\n\tv_exp_f32 v83, v49\n\t"
        "v_cvt_pk_bf16_f32 v55, v44, v45\n\ts_cbranch_scc1 4f\n\ts_waitcnt lgkmcnt(3)\n\tv_mfma_f32_32x32x16_bf16 v[34:49], v[72:75], v[50:53], 0\n\t"
        "ds_read_b64_tr_b16 v[64:65], v84 offset:9216\n\tds_read_b64_tr_b16 v[66:67], v84 offset:9600\n\tv_exp_f32 v18, v18\n\tv_exp_f32 v19, v19\n\t"
        "v_cvt_pk_bf16_f32 v56, v80, v81\n\tv_exp_f32 v20, v20\n\tv_exp_f32 v21, v21\n\tv_cvt_pk_bf16_f32 v57, v82, v83\n\tv_exp_f32 v22, v22\n\t"
        "s_waitcnt lgkmcnt(2)\n\tv_mfma_f32_32x32x16_bf16 v[2:17], v[68:71], v[54:57], v[2:17]\n\tv_exp_f32 v23, v23\n\tv_cvt_pk_bf16_f32 v58, v18, v19\n\t"
        "v_exp_f32 v24, v24\n\tv_exp_f32 v25, v25\n\tv_cvt_pk_bf16_f32 v59, v20, v21\n\tv_cmp_gt_u32 vcc, %[need], v88\n\ts_cbranch_vccnz 24f\n\t14:\n\t"
        "v_mfma_f32_32x32x8_bf16 v[34:49], v[62:63], v[78:79], v[34:49]\n\tds_read_b128 v[72:75], v76 offset:6144\n\tds_read_b64 v[62:63], v77 offset:6144\n\t"
        "s_cmp_eq_u32 %[pexit], 2\n\tv_exp_f32 v26, v26\n\tv_exp_f32 v27, v27\n\tv_cvt_pk_bf16_f32 v60, v22, v23\n\tv_exp_f32 v28, v28\n\t"
        "v_exp_f32 v29, v29\n\tv_cvt_pk_bf16_f32 v61, v24, v25\n\tv_exp_f32 v80, v30\n\ts_waitcnt lgkmcnt(2)\n\t"
        "v_mfma_f32_32x32x16_bf16 v[2:17], v[64:67], v[58:61], v[2:17]\n\tds_read_b32 v88, v87 offset:12\n\tds_read_b64_tr_b16 v[68:69], v84 offset:9984\n\t"
        "ds_read_b64_tr_b16 v[70:71], v84 offset:10368\n\tv_exp_f32 v81, v31\n\tv_cvt_pk_bf16_f32 v54, v26, v27\n\tv_exp_f32 v82, v32\n\tv_exp_f32 v83, v33\n\t"
        "v_cvt_pk_bf16_f32 v55, v28, v29\n\ts_cbranch_scc1 4f\n\ts_waitcnt lgkmcnt(3)\n\tv_mfma_f32_32x32x16_bf16 v[18:33], v[72:75], v[50:53], 0\n\t"
        "ds_read_b64_tr_b16 v[64:65], v84 offset:7680\n\tds_read_b64_tr_b16 v[66:67], v84 offset:8064\n\tv_exp_f32 v34, v34\n\tv_exp_f32 v35, v35\n\t"
        "v_cvt_pk_bf16_f32 v56, v80, v81\n\tv_exp_f32 v36, v36\n\tv_exp_f32 v37, v37\n\tv_cvt_pk_bf16_f32 v57, v82, v83\n\tv_exp_f32 v38, v38\n\t"
        "s_waitcnt lgkmcnt(2)\n\tv_mfma_f32_32x32x16_bf16 v[2:17], v[68:71], v[54:57], v[2:17]\n\tv_exp_f32 v39, v39\n\tv_cvt_pk_bf16_f32 v58, v34, v35\n\t"
        "v_exp_f32 v40, v40\n\tv_exp_f32 v41, v41\n\tv_cvt_pk_bf16_f32 v59, v36, v37\n\tv_cmp_gt_u32 vcc, %[need], v88\n\ts_cbranch_vccnz 23f\n\t13:\n\t"
        "v_mfma_f32_32x32x8_bf16 v[18:33], v[62:63], v[78:79], v[18:33]\n\tds_read_b128 v[72:75], v76 offset:4608\n\tds_read_b64 v[62:63], v77 offset:4608\n\t"
        "s_cmp_eq_u32 %[pexit], 3\n\tv_exp_f32 v42, v42\n\tv_exp_f32 v43, v43\n\tv_cvt_pk_bf16_f32 v60, v38, v39\n\tv_exp_f32 v44, v44\n\t"
        "v_exp_f32 v45, v45\n\tv_cvt_pk_bf16_f32 v61, v40, v41\n\tv_exp_f32 v80, v46\n\ts_waitcnt lgkmcnt(2)\n\t"
        "v_mfma_f32_32x32x16_bf16 v[2:17], v[64:67], v[58:61], v[2:17]\n\tds_read_b32 v88, v87 offset:8\n\tds_read_b64_tr_b16 v[68:69], v84 offset:8448\n\t"
        "ds_read_b64_tr_b16 v[70:71], v84 offset:8832\n\tv_exp_f32 v81, v47\n\tv_cvt_pk_bf16_f32 v54, v42, v43\n\tv_exp_f32 v82, v48\n\tv_exp_f32 v83, v49\n\t"
        "v_cvt_pk_bf16_f32 v55, v44, v45\n\ts_cbranch_scc1 4f\n\ts_waitcnt lgkmcnt(3)\n\tv_mfma_f32_32x32x16_bf16 v[34:49], v[72:75], v[50:53], 0\n\t"
        "ds_read_b64_tr_b16 v[64:65], v84 offset:6144\n\tds_read_b64_tr_b16 v[66:67], v84 offset:6528\n\tv_exp_f32 v18, v18\n\tv_exp_f32 v19, v19\n\t"
        "v_cvt_pk_bf16_f32 v56, v80, v81\n\tv_exp_f32 v20, v20\n\tv_exp_f32 v21, v21\n\tv_cvt_pk_bf16_f32 v57, v82, v83\n\tv_exp_f32 v22, v22\n\t"
        "s_waitcnt lgkmcnt(2)\n\tv_mfma_f32_32x32x16_bf16 v[2:17], v[68:71], v[54:57], v[2:17]\n\tv_exp_f32 v23, v23\n\tv_cvt_pk_bf16_f32 v58, v18, v19\n\t"
        "v_exp_f32 v24, v24\n\tv_exp_f32 v25, v25\n\tv_cvt_pk_bf16_f32 v59, v20, v21\n\tv_cmp_gt_u32 vcc, %[need], v88\n\ts_cbranch_vccnz 22f\n\t12:\n\t"
        "v_mfma_f32_32x32x8_bf16 v[34:49], v[62:63], v[78:79], v[34:49]\n\tds_read_b128 v[72:75], v76 offset:3072\n\tds_read_b64 v[62:63], v77 offset:3072\n\t"
        "s_cmp_eq_u32 %[pexit], 4\n\tv_exp_f32 v26, v26\n\tv_exp_f32 v27, v27\n\tv_cvt_pk_bf16_f32 v60, v22, v23\n\tv_exp_f32 v28, v28\n\t"
        "v_exp_f32 v29, v29\n\tv_cvt_pk_bf16_f32 v61, v24, v25\n\tv_exp_f32 v80, v30\n\ts_waitcnt lgkmcnt(2)\n\t"
        "v_mfma_f32_32x32x16_bf16 v[2:17], v[64:67], v[58:61], v[2:17]\n\tds_read_b32 v88, v87 offset:4\n\tds_read_b64_tr_b16 v[68:69], v84 offset:6912\n\t"
        "ds_read_b64_tr_b16 v[70:71], v84 offset:7296\n\tv_exp_f32 v81, v31\n\tv_cvt_pk_bf16_f32 v54, v26, v27\n\tv_exp_f32 v82, v32\n\tv_exp_f32 v83, v33\n\t"
        "v_cvt_pk_bf16_f32 v55, v28, v29\n\ts_cbranch_scc1 4f\n\ts_waitcnt lgkmcnt(3)\n\tv_mfma_f32_32x32x16_bf16 v[18:33], v[72:75], v[50:53], 0\n\t"
        "ds_read_b64_tr_b16 v[64:65], v84 offset:4608\n\tds_read_b64_tr_b16 v[66:67], v84 offset:4992\n\tv_exp_f32 v34, v34\n\tv_exp_f32 v35, v35\n\t"
        "v_cvt_pk_bf16_f32 v56, v80, v81\n\tv_exp_f32 v36, v36\n\tv_exp_f32 v37, v37\n\tv_cvt_pk_bf16_f32 v57, v82, v83\n\tv_exp_f32 v38, v38\n\t"
        "s_waitcnt lgkmcnt(2)\n\tv_mfma_f32_32x32x16_bf16 v[2:17], v[68:71], v[54:57], v[2:17]\n\tv_exp_f32 v39, v39\n\tv_cvt_pk_bf16_f32 v58, v34, v35\n\t"
        "v_exp_f32 v40, v40\n\tv_exp_f32 v41, v41\n\tv_cvt_pk_bf16_f32 v59, v36, v37\n\tv_cmp_gt_u32 vcc, %[need], v88\n\ts_cbranch_vccnz 21f\n\t11:\n\t"
        "v_mfma_f32_32x32x8_bf16 v[18:33], v[62:63], v[78:79], v[18:33]\n\tds_read_b128 v[72:75], v76 offset:1536\n\tds_read_b64 v[62:63], v77 offset:1536\n\t"
        "s_cmp_eq_u32 %[pexit], 5\n\tv_exp_f32 v42, v42\n\tv_exp_f32 v43, v43\n\tv_cvt_pk_bf16_f32 v60, v38, v39\n\tv_exp_f32 v44, v44\n\t"
        "v_exp_f32 v45, v45\n\tv_cvt_pk_bf16_f32 v61, v40, v41\n\tv_exp_f32 v80, v46\n\ts_waitcnt lgkmcnt(2)\n\t"
        "v_mfma_f32_32x32x16_bf16 v[2:17], v[64:67], v[58:61], v[2:17]\n\tds_read_b32 v88, v87 offset:0\n\tds_read_b64_tr_b16 v[68:69], v84 offset:5376\n\t"
        "ds_read_b64_tr_b16 v[70:71], v84 offset:5760\n\tv_exp_f32 v81, v47\n\tv_cvt_pk_bf16_f32 v54, v42, v43\n\tv_exp_f32 v82, v48\n\tv_exp_f32 v83, v49\n\t"
        "v_cvt_pk_bf16_f32 v55, v44, v45\n\ts_cbranch_scc1 4f\n\ts_waitcnt lgkmcnt(3)\n\tv_mfma_f32_32x32x16_bf16 v[34:49], v[72:75], v[50:53], 0\n\t"
        "ds_read_b64_tr_b16 v[64:65], v84 offset:3072\n\tds_read_b64_tr_b16 v[66:67], v84 offset:3456\n\tv_exp_f32 v18, v18\n\tv_exp_f32 v19, v19\n\t"
        "v_cvt_pk_bf16_f32 v56, v80, v81\n\tv_exp_f32 v20, v20\n\tv_exp_f32 v21, v21\n\tv_cvt_pk_bf16_f32 v57, v82, v83\n\tv_exp_f32 v22, v22\n\t"
        "s_waitcnt lgkmcnt(2)\n\tv_mfma_f32_32x32x16_bf16 v[2:17], v[68:71], v[54:57], v[2:17]\n\tv_exp_f32 v23, v23\n\tv_cvt_pk_bf16_f32 v58, v18, v19\n\t"
        "v_exp_f32 v24, v24\n\tv_exp_f32 v25, v25\n\tv_cvt_pk_bf16_f32 v59, v20, v21\n\tv_cmp_gt_u32 vcc, %[need], v88\n\ts_cbranch_vccnz 20f\n\t10:\n\t"
        "v_mfma_f32_32x32x8_bf16 v[34:49], v[62:63], v[78:79], v[34:49]\n\tds_read_b128 v[72:75], v76 offset:0\n\tds_read_b64 v[62:63], v77 offset:0\n\t"
        "s_cmp_eq_u32 %[pexit], 6\n\tv_exp_f32 v26, v26\n\tv_exp_f32 v27, v27\n\tv_cvt_pk_bf16_f32 v60, v22, v23\n\tv_exp_f32 v28, v28\n\t"
        "v_exp_f32 v29, v29\n\tv_cvt_pk_bf16_f32 v61, v24, v25\n\tv_exp_f32 v80, v30\n\ts_waitcnt lgkmcnt(2)\n\t"
        "v_mfma_f32_32x32x16_bf16 v[2:17], v[64:67], v[58:61], v[2:17]\n\tds_read_b64_tr_b16 v[68:69], v84 offset:3840\n\t"
        "ds_read_b64_tr_b16 v[70:71], v84 offset:4224\n\tv_exp_f32 v81, v31\n\tv_cvt_pk_bf16_f32 v54, v26, v27\n\tv_exp_f32 v82, v32\n\tv_exp_f32 v83, v33\n\t"
        "v_cvt_pk_bf16_f32 v55, v28, v29\n\ts_cbranch_scc1 4f\n\ts_waitcnt lgkmcnt(3)\n\tv_mfma_f32_32x32x16_bf16 v[18:33], v[72:75], v[50:53], v[90:105]\n\t"
        "ds_read_b64_tr_b16 v[64:65], v84 offset:1536\n\tds_read_b64_tr_b16 v[66:67], v84 offset:1920\n\tv_exp_f32 v34, v34\n\tv_exp_f32 v35, v35\n\t"
        "v_cvt_pk_bf16_f32 v56, v80, v81\n\tv_exp_f32 v36, v36\n\tv_exp_f32 v37, v37\n\tv_cvt_pk_bf16_f32 v57, v82, v83\n\tv_exp_f32 v38, v38\n\t"
        "s_waitcnt lgkmcnt(2)\n\tv_mfma_f32_32x32x16_bf16 v[2:17], v[68:71], v[54:57], v[2:17]\n\tv_exp_f32 v39, v39\n\tv_cvt_pk_bf16_f32 v58, v34, v35\n\t"
        "v_exp_f32 v40, v40\n\tv_exp_f32 v41, v41\n\tv_cvt_pk_bf16_f32 v59, v36, v37\n\tv_mfma_f32_32x32x8_bf16 v[18:33], v[62:63], v[78:79], v[18:33]\n\t"
        "s_cmp_eq_u32 %[pexit], 7\n\tv_exp_f32 v42, v42\n\tv_exp_f32 v43, v43\n\tv_cvt_pk_bf16_f32 v60, v38, v39\n\tv_exp_f32 v44, v44\n\t"
        "v_exp_f32 v45, v45\n\tv_cvt_pk_bf16_f32 v61, v40, v41\n\tv_exp_f32 v80, v46\n\ts_waitcnt lgkmcnt(0)\n\t"
        "v_mfma_f32_32x32x16_bf16 v[2:17], v[64:67], v[58:61], v[2:17]\n\tds_read_b64_tr_b16 v[68:69], v84 offset:2304\n\t"
        "ds_read_b64_tr_b16 v[70:71], v84 offset:2688\n\tv_exp_f32 v81, v47\n\tv_cvt_pk_bf16_f32 v54, v42, v43\n\tv_exp_f32 v82, v48\n\tv_exp_f32 v83, v49\n\t"
        "v_cvt_pk_bf16_f32 v55, v44, v45\n\ts_cbranch_scc1 4f\n\ts_waitcnt lgkmcnt(3)\n\tds_read_b64_tr_b16 v[64:65], v84 offset:0\n\t"
        "ds_read_b64_tr_b16 v[66:67], v84 offset:384\n\tv_exp_f32 v18, v18\n\tv_exp_f32 v19, v19\n\tv_cvt_pk_bf16_f32 v56, v80, v81\n\tv_exp_f32 v20, v20\n\t"
        "v_exp_f32 v21, v21\n\tv_cvt_pk_bf16_f32 v57, v82, v83\n\tv_exp_f32 v22, v22\n\ts_waitcnt lgkmcnt(2)\n\t"
        "v_mfma_f32_32x32x16_bf16 v[2:17], v[68:71], v[54:57], v[2:17]\n\tv_exp_f32 v23, v23\n\tv_cvt_pk_bf16_f32 v58, v18, v19\n\tv_exp_f32 v24, v24\n\t"
        "v_exp_f32 v25, v25\n\tv_cvt_pk_bf16_f32 v59, v20, v21\n\tv_exp_f32 v26, v26\n\tv_exp_f32 v27, v27\n\tv_cvt_pk_bf16_f32 v60, v22, v23\n\t"
        "v_exp_f32 v28, v28\n\tv_exp_f32 v29, v29\n\tv_cvt_pk_bf16_f32 v61, v24, v25\n\tv_exp_f32 v80, v30\n\ts_waitcnt lgkmcnt(0)\n\t"
        "v_mfma_f32_32x32x16_bf16 v[2:17], v[64:67], v[58:61], v[2:17]\n\tds_read_b64_tr_b16 v[68:69], v84 offset:768\n\t"
        "ds_read_b64_tr_b16 v[70:71], v84 offset:1152\n\tv_exp_f32 v81, v31\n\tv_cvt_pk_bf16_f32 v54, v26, v27\n\tv_exp_f32 v82, v32\n\tv_exp_f32 v83, v33\n\t"
        "v_cvt_pk_bf16_f32 v55, v28, v29\n\ts_branch 4f\n\t20:\n\ts_cmp_eq_u32 %[bud], 0\n\ts_cbranch_scc1 10b\n\ts_sub_u32 %[bud], %[bud], 1\n\ts_sleep 1\n\t"
        "ds_read_b32 v88, v87 offset:0\n\ts_waitcnt lgkmcnt(0)\n\tv_cmp_gt_u32 vcc, %[need], v88\n\ts_cbranch_vccnz 20b\n\ts_branch 10b\n\t21:\n\t"
        "s_cmp_eq_u32 %[bud], 0\n\ts_cbranch_scc1 11b\n\ts_sub_u32 %[bud], %[bud], 1\n\ts_sleep 1\n\tds_read_b32 v88, v87 offset:4\n\ts_waitcnt lgkmcnt(0)\n\t"
        "v_cmp_gt_u32 vcc, %[need], v88\n\ts_cbranch_vccnz 21b\n\ts_branch 11b\n\t22:\n\ts_cmp_eq_u32 %[bud], 0\n\ts_cbranch_scc1 12b\n\t"
        "s_sub_u32 %[bud], %[bud], 1\n\ts_sleep 1\n\tds_read_b32 v88, v87 offset:8\n\ts_waitcnt lgkmcnt(0)\n\tv_cmp_gt_u32 vcc, %[need], v88\n\t"
        "s_cbranch_vccnz 22b\n\ts_branch 12b\n\t23:\n\ts_cmp_eq_u32 %[bud], 0\n\ts_cbranch_scc1 13b\n\ts_sub_u32 %[bud], %[bud], 1\n\ts_sleep 1\n\t"
        "ds_read_b32 v88, v87 offset:12\n\ts_waitcnt lgkmcnt(0)\n\tv_cmp_gt_u32 vcc, %[need], v88\n\ts_cbranch_vccnz 23b\n\ts_branch 13b\n\t24:\n\t"
        "s_cmp_eq_u32 %[bud], 0\n\ts_cbranch_scc1 14b\n\ts_sub_u32 %[bud], %[bud], 1\n\ts_sleep 1\n\tds_read_b32 v88, v87 offset:16\n\ts_waitcnt lgkmcnt(0)\n\t"
        "v_cmp_gt_u32 vcc, %[need], v88\n\ts_cbranch_vccnz 24b\n\ts_branch 14b\n\t25:\n\ts_cmp_eq_u32 %[bud], 0\n\ts_cbranch_scc1 15b\n\t"
        "s_sub_u32 %[bud], %[bud], 1\n\ts_sleep 1\n\tds_read_b32 v88, v87 offset:20\n\ts_waitcnt lgkmcnt(0)\n\tv_cmp_gt_u32 vcc, %[need], v88\n\t"
        "s_cbranch_vccnz 25b\n\ts_branch 15b\n\t26:\n\ts_cmp_eq_u32 %[bud], 0\n\ts_cbranch_scc1 16b\n\ts_sub_u32 %[bud], %[bud], 1\n\ts_sleep 1\n\t"
        "ds_read_b32 v88, v87 offset:24\n\ts_waitcnt lgkmcnt(0)\n\tv_cmp_gt_u32 vcc, %[need], v88\n\ts_cbranch_vccnz 26b\n\ts_branch 16b\n\t27:\n\t"
        "s_cmp_eq_u32 %[bud], 0\n\ts_cbranch_scc1 17b\n\ts_sub_u32 %[bud], %[bud], 1\n\ts_sleep 1\n\tds_read_b32 v88, v87 offset:28\n\ts_waitcnt lgkmcnt(0)\n\t"
        "v_cmp_gt_u32 vcc, %[need], v88\n\ts_cbranch_vccnz 27b\n\ts_branch 17b\n\t4:\n\tv_cvt_pk_bf16_f32 v56, v80, v81\n\tv_cvt_pk_bf16_f32 v57, v82, v83\n\t"
        "s_waitcnt lgkmcnt(0)\n\ts_nop 1\n\tv_mfma_f32_32x32x16_bf16 v[2:17], v[68:71], v[54:57], v[2:17]\n\ts_setprio 0\n\ts_nop 10\n\t"
        : "=&{v[2:17]}"(o), "+{v[18:33]}"(s0), [bud] "+&s"(budget)
        : "{v[50:53]}"(qm), "{v[78:79]}"(qtl), "{v[90:105]}"(mask), "{v76}"(ka), "{v77}"(kta), "{v84}"(va), "{v87}"(rdya), [need] "s"(need),
          [pexit] "s"(pexit)
        : "v34", "v35", "v36", "v37", "v38", "v39", "v40", "v41", "v42", "v43", "v44", "v45", "v46", "v47", "v48", "v49", "v54", "v55", "v56", "v57",
          "v58", "v59", "v60", "v61", "v62", "v63", "v64", "v65", "v66", "v67", "v68", "v69", "v70", "v71", "v72", "v73", "v74", "v75", "v80", "v81",
          "v82", "v83", "v88",
          "vcc", "scc", "memory");
    return o;
}

DI void qkv_tile(u32x4& qm, u32x2& qt, f32x16& s0, unsigned qda, unsigned qdv, unsigned qpa, const char* xsrc, const char* ssrc, unsigned wa, unsigned wta, unsigned xoff, unsigned rdya, unsigned soff, unsigned da,
                 unsigned db, unsigned aga, unsigned wb, unsigned qv, char* kvd, unsigned ga, unsigned gb,
                 unsigned& budget) {
    budget = __builtin_amdgcn_readfirstlane(budget);
    xsrc += 4096;
    asm volatile(
        "s_nop 4\n\tglobal_load_dwordx4 v[66:69], v108, s[20:21] offset:-4096 sc1\n\tglobal_load_dwordx4 v[70:73], v108, s[20:21] offset:-3072 sc1\n\t"
        "global_load_dwordx2 v[116:117], v111, %[ssrc] offset:0 sc1\n\tglobal_load_dwordx2 v[118:119], v111, %[ssrc] offset:128 sc1\n\t"
        "global_load_dwordx2 v[120:121], v111, %[ssrc] offset:256 sc1\n\tglobal_load_dwordx2 v[122:123], v111, %[ssrc] offset:384 sc1\n\t"
        "global_load_dwordx4 v[74:77], v108, s[20:21] offset:-2048 sc1\n\tglobal_load_dwordx4 v[78:81], v108, s[20:21] offset:-1024 sc1\n\t"
        "global_load_dwordx4 v[82:85], v108, s[20:21] offset:0 sc1\n\tglobal_load_dwordx4 v[86:89], v108, s[20:21] offset:1024 sc1\n\t"
        "global_load_dwordx4 v[90:93], v108, s[20:21] offset:2048 sc1\n\tglobal_load_dwordx4 v[94:97], v108, s[20:21] offset:3072 sc1\n\t"
        "s_add_u32 s20, s20, 8192\n\ts_addc_u32 s21, s21, 0\n\tglobal_load_dwordx4 v[98:101], v108, s[20:21] offset:-4096 sc1\n\t"
        "global_load_dwordx4 v[102:105], v108, s[20:21] offset:-3072 sc1\n\tds_read_b32 v110, v109\n\ts_add_u32 s23, %[wb], 2\n\ts_waitcnt lgkmcnt(0)\n\t"
        "v_cmp_gt_u32 vcc, s23, v110\n\ts_cbranch_vccnz 20f\n\t10:\n\tds_read_b128 v[34:37], v106 offset:0\n\tds_read_b128 v[38:41], v107 offset:0\n\t"
        "ds_read_b128 v[42:45], v106 offset:1920\n\tds_read_b128 v[46:49], v107 offset:1920\n\tds_read_b128 v[50:53], v106 offset:3840\n\t"
        "ds_read_b128 v[54:57], v107 offset:3840\n\tds_read_b128 v[58:61], v106 offset:5760\n\tds_read_b128 v[62:65], v107 offset:5760\n\t"
        "s_waitcnt vmcnt(13) lgkmcnt(7)\n\tv_mfma_f32_32x32x16_bf16 v[2:17], v[34:37], v[66:69], 0\n\tds_read_b128 v[34:37], v106 offset:7680\n\t"
        "s_waitcnt lgkmcnt(7)\n\tv_mfma_f32_32x32x16_bf16 v[18:33], v[38:41], v[66:69], 0\n\tds_read_b128 v[38:41], v107 offset:7680\n\t"
        "global_load_dwordx4 v[66:69], v108, s[20:21] offset:-2048 sc1\n\ts_waitcnt vmcnt(13) lgkmcnt(7)\n\t"
        "v_mfma_f32_32x32x16_bf16 v[2:17], v[42:45], v[70:73], v[2:17]\n\tds_read_b128 v[42:45], v106 offset:9600\n\ts_waitcnt lgkmcnt(7)\n\t"
        "v_mfma_f32_32x32x16_bf16 v[18:33], v[46:49], v[70:73], v[18:33]\n\tds_read_b128 v[46:49], v107 offset:9600\n\t"
        "global_load_dwordx4 v[70:73], v108, s[20:21] offset:-1024 sc1\n\ts_waitcnt vmcnt(9) lgkmcnt(7)\n\t"
        "v_mfma_f32_32x32x16_bf16 v[2:17], v[50:53], v[74:77], v[2:17]\n\tds_read_b128 v[50:53], v106 offset:11520\n\tv_add_f32 v116, v116, v120\n\t"
        "v_add_f32 v117, v117, v121\n\tv_add_f32 v118, v118, v122\n\tv_add_f32 v119, v119, v123\n\tv_add_f32 v116, v116, v118\n\tds_read_b32 v110, v109\n\t"
        "s_add_u32 s23, %[wb], 4\n\ts_waitcnt lgkmcnt(8)\n\tv_mfma_f32_32x32x16_bf16 v[18:33], v[54:57], v[74:77], v[18:33]\n\t"
        "ds_read_b128 v[54:57], v107 offset:11520\n\tv_add_f32 v117, v117, v119\n\tv_mov_b32 v118, v116\n\tv_mov_b32 v119, v117\n\ts_nop 1\n\t"
        "v_permlane32_swap_b32 v116, v118\n\tglobal_load_dwordx4 v[74:77], v108, s[20:21] offset:0 sc1\n\ts_waitcnt vmcnt(9) lgkmcnt(8)\n\t"
        "v_mfma_f32_32x32x16_bf16 v[2:17], v[58:61], v[78:81], v[2:17]\n\tds_read_b128 v[58:61], v106 offset:13440\n\tv_permlane32_swap_b32 v117, v119\n\t"
        "s_nop 0\n\tv_add_f32 v116, v116, v118\n\tv_add_f32 v117, v117, v119\n\tv_mul_f32 v116, %[ninvd], v116\n\ts_waitcnt lgkmcnt(8)\n\t"
        "v_mfma_f32_32x32x16_bf16 v[18:33], v[62:65], v[78:81], v[18:33]\n\tds_read_b128 v[62:65], v107 offset:13440\n\tv_mul_f32 v117, %[ninvd], v117\n\t"
        "v_fma_f32 v117, -v116, v116, -v117\n\tv_max_f32 v117, 0, v117\n\tv_add_f32 v117, %[eps], v117\n\tv_rsq_f32 v114, v117\n\t"
        "global_load_dwordx4 v[78:81], v108, s[20:21] offset:1024 sc1\n\ts_waitcnt vmcnt(9) lgkmcnt(3)\n\tv_cmp_gt_u32 vcc, s23, v110\n\t"
        "s_cbranch_vccnz 21f\n\t11:\n\tv_mfma_f32_32x32x16_bf16 v[2:17], v[34:37], v[82:85], v[2:17]\n\tds_read_b128 v[34:37], v106 offset:15360\n\t"
        "v_cvt_pk_bf16_f32 v113, v116, 0\n\tv_lshlrev_b32 v118, 16, v113\n\tv_mul_f32 v115, %[qsc], v114\n\tv_sub_f32 v117, v116, v118\n\t"
        "v_cvt_pk_bf16_f32 v112, v118, v117\n\ts_nop 0\n\tv_mfma_f32_32x32x16_bf16 v[18:33], v[38:41], v[82:85], v[18:33]\n\t"
        "ds_read_b128 v[38:41], v107 offset:15360\n\tglobal_load_dwordx4 v[82:85], v108, s[20:21] offset:2048 sc1\n\ts_waitcnt vmcnt(9)\n\t"
        "v_mfma_f32_32x32x16_bf16 v[2:17], v[42:45], v[86:89], v[2:17]\n\tds_read_b128 v[42:45], v106 offset:17280\n\ts_nop 0\n\t"
        "v_mfma_f32_32x32x16_bf16 v[18:33], v[46:49], v[86:89], v[18:33]\n\tds_read_b128 v[46:49], v107 offset:17280\n\t"
        "global_load_dwordx4 v[86:89], v108, s[20:21] offset:3072 sc1\n\ts_waitcnt vmcnt(9)\n\t"
        "v_mfma_f32_32x32x16_bf16 v[2:17], v[50:53], v[90:93], v[2:17]\n\tds_read_b128 v[50:53], v106 offset:19200\n\ts_waitcnt lgkmcnt(7)\n\t"
        "v_mfma_f32_32x32x16_bf16 v[18:33], v[54:57], v[90:93], v[18:33]\n\tds_read_b128 v[54:57], v107 offset:19200\n\ts_add_u32 s20, s20, 8192\n\t"
        "s_addc_u32 s21, s21, 0\n\tglobal_load_dwordx4 v[90:93], v108, s[20:21] offset:-4096 sc1\n\ts_waitcnt vmcnt(9) lgkmcnt(7)\n\t"
        "v_mfma_f32_32x32x16_bf16 v[2:17], v[58:61], v[94:97], v[2:17]\n\tds_read_b128 v[58:61], v106 offset:21120\n\ts_waitcnt lgkmcnt(7)\n\t"
        "v_mfma_f32_32x32x16_bf16 v[18:33], v[62:65], v[94:97], v[18:33]\n\tds_read_b128 v[62:65], v107 offset:21120\n\t"
        "global_load_dwordx4 v[94:97], v108, s[20:21] offset:-3072 sc1\n\ts_waitcnt vmcnt(9) lgkmcnt(7)\n\t"
        "v_mfma_f32_32x32x16_bf16 v[2:17], v[34:37], v[98:101], v[2:17]\n\tds_read_b128 v[34:37], v106 offset:23040\n\ts_waitcnt lgkmcnt(7)\n\t"
        "v_mfma_f32_32x32x16_bf16 v[18:33], v[38:41], v[98:101], v[18:33]\n\tds_read_b128 v[38:41], v107 offset:23040\n\t"
        "global_load_dwordx4 v[98:101], v108, s[20:21] offset:-2048 sc1\n\ts_waitcnt vmcnt(9) lgkmcnt(7)\n\t"
        "v_mfma_f32_32x32x16_bf16 v[2:17], v[42:45], v[102:105], v[2:17]\n\tds_read_b128 v[42:45], v106 offset:24960\n\ts_waitcnt lgkmcnt(7)\n\t"
        "v_mfma_f32_32x32x16_bf16 v[18:33], v[46:49], v[102:105], v[18:33]\n\tds_read_b128 v[46:49], v107 offset:24960\n\t"
        "global_load_dwordx4 v[102:105], v108, s[20:21] offset:-1024 sc1\n\ts_waitcnt vmcnt(9) lgkmcnt(7)\n\t"
        "v_mfma_f32_32x32x16_bf16 v[2:17], v[50:53], v[66:69], v[2:17]\n\tds_read_b128 v[50:53], v106 offset:26880\n\tds_read_b32 v110, v109\n\t"
        "s_add_u32 s23, %[wb], 6\n\ts_waitcnt lgkmcnt(8)\n\tv_mfma_f32_32x32x16_bf16 v[18:33], v[54:57], v[66:69], v[18:33]\n\t"
        "ds_read_b128 v[54:57], v107 offset:26880\n\tglobal_load_dwordx4 v[66:69], v108, s[20:21] offset:0 sc1\n\ts_waitcnt vmcnt(9) lgkmcnt(8)\n\t"
        "v_mfma_f32_32x32x16_bf16 v[2:17], v[58:61], v[70:73], v[2:17]\n\tds_read_b128 v[58:61], v106 offset:28800\n\ts_waitcnt lgkmcnt(8)\n\t"
        "v_mfma_f32_32x32x16_bf16 v[18:33], v[62:65], v[70:73], v[18:33]\n\tds_read_b128 v[62:65], v107 offset:28800\n\t"
        "global_load_dwordx4 v[70:73], v108, s[20:21] offset:1024 sc1\n\ts_waitcnt vmcnt(9) lgkmcnt(3)\n\tv_cmp_gt_u32 vcc, s23, v110\n\t"
        "s_cbranch_vccnz 22f\n\t12:\n\tv_mfma_f32_32x32x16_bf16 v[2:17], v[34:37], v[74:77], v[2:17]\n\tds_read_b128 v[34:37], v106 offset:30720\n\ts_nop 0\n\t"
        "v_mfma_f32_32x32x16_bf16 v[18:33], v[38:41], v[74:77], v[18:33]\n\tds_read_b128 v[38:41], v107 offset:30720\n\t"
        "global_load_dwordx4 v[74:77], v108, s[20:21] offset:2048 sc1\n\ts_waitcnt vmcnt(9)\n\t"
        "v_mfma_f32_32x32x16_bf16 v[2:17], v[42:45], v[78:81], v[2:17]\n\tds_read_b128 v[42:45], v106 offset:32640\n\ts_nop 0\n\t"
        "v_mfma_f32_32x32x16_bf16 v[18:33], v[46:49], v[78:81], v[18:33]\n\tds_read_b128 v[46:49], v107 offset:32640\n\t"
        "global_load_dwordx4 v[78:81], v108, s[20:21] offset:3072 sc1\n\ts_waitcnt vmcnt(9)\n\t"
        "v_mfma_f32_32x32x16_bf16 v[2:17], v[50:53], v[82:85], v[2:17]\n\tv_add_u32 v106, 30720, v106\n\tv_add_u32 v107, 30720, v107\n\t"
        "ds_read_b128 v[50:53], v106 offset:3840\n\ts_waitcnt lgkmcnt(7)\n\tv_mfma_f32_32x32x16_bf16 v[18:33], v[54:57], v[82:85], v[18:33]\n\t"
        "ds_read_b128 v[54:57], v107 offset:3840\n\ts_add_u32 s20, s20, 8192\n\ts_addc_u32 s21, s21, 0\n\t"
        "global_load_dwordx4 v[82:85], v108, s[20:21] offset:-4096 sc1\n\ts_waitcnt vmcnt(9) lgkmcnt(7)\n\t"
        "v_mfma_f32_32x32x16_bf16 v[2:17], v[58:61], v[86:89], v[2:17]\n\tds_read_b128 v[58:61], v106 offset:5760\n\ts_waitcnt lgkmcnt(7)\n\t"
        "v_mfma_f32_32x32x16_bf16 v[18:33], v[62:65], v[86:89], v[18:33]\n\tds_read_b128 v[62:65], v107 offset:5760\n\t"
        "global_load_dwordx4 v[86:89], v108, s[20:21] offset:-3072 sc1\n\ts_waitcnt vmcnt(9) lgkmcnt(7)\n\t"
        "v_mfma_f32_32x32x16_bf16 v[2:17], v[34:37], v[90:93], v[2:17]\n\tds_read_b128 v[34:37], v106 offset:7680\n\ts_waitcnt lgkmcnt(7)\n\t"
        "v_mfma_f32_32x32x16_bf16 v[18:33], v[38:41], v[90:93], v[18:33]\n\tds_read_b128 v[38:41], v107 offset:7680\n\t"
        "global_load_dwordx4 v[90:93], v108, s[20:21] offset:-2048 sc1\n\ts_waitcnt vmcnt(9) lgkmcnt(7)\n\t"
        "v_mfma_f32_32x32x16_bf16 v[2:17], v[42:45], v[94:97], v[2:17]\n\tds_read_b128 v[42:45], v106 offset:9600\n\ts_add_u32 s22, %[wb], 1\n\t"
        "v_mov_b32 v120, %[qpa]\n\tv_mov_b32 v121, s22\n\tds_write_b32 v120, v121\n\ts_waitcnt lgkmcnt(8)\n\t"
        "v_mfma_f32_32x32x16_bf16 v[18:33], v[46:49], v[94:97], v[18:33]\n\tds_read_b128 v[46:49], v107 offset:9600\n\t"
        "global_load_dwordx4 v[94:97], v108, s[20:21] offset:-1024 sc1\n\ts_waitcnt vmcnt(9) lgkmcnt(8)\n\t"
        "v_mfma_f32_32x32x16_bf16 v[2:17], v[50:53], v[98:101], v[2:17]\n\tds_read_b128 v[50:53], v106 offset:11520\n\tds_read_b32 v110, v109\n\t"
        "s_add_u32 s23, %[wb], 8\n\ts_waitcnt lgkmcnt(9)\n\tv_mfma_f32_32x32x16_bf16 v[18:33], v[54:57], v[98:101], v[18:33]\n\t"
        "ds_read_b128 v[54:57], v107 offset:11520\n\tglobal_load_dwordx4 v[98:101], v108, s[20:21] offset:0 sc1\n\ts_waitcnt vmcnt(9) lgkmcnt(9)\n\t"
        "v_mfma_f32_32x32x16_bf16 v[2:17], v[58:61], v[102:105], v[2:17]\n\tds_read_b128 v[58:61], v106 offset:13440\n\ts_waitcnt lgkmcnt(9)\n\t"
        "v_mfma_f32_32x32x16_bf16 v[18:33], v[62:65], v[102:105], v[18:33]\n\tds_read_b128 v[62:65], v107 offset:13440\n\t"
        "global_load_dwordx4 v[102:105], v108, s[20:21] offset:1024 sc1\n\ts_waitcnt vmcnt(9) lgkmcnt(3)\n\tv_cmp_gt_u32 vcc, s23, v110\n\t"
        "s_cbranch_vccnz 23f\n\t13:\n\tv_mfma_f32_32x32x16_bf16 v[2:17], v[34:37], v[66:69], v[2:17]\n\tds_read_b128 v[34:37], v106 offset:15360\n\ts_nop 0\n\t"
        "v_mfma_f32_32x32x16_bf16 v[18:33], v[38:41], v[66:69], v[18:33]\n\tds_read_b128 v[38:41], v107 offset:15360\n\t"
        "global_load_dwordx4 v[66:69], v108, s[20:21] offset:2048 sc1\n\ts_waitcnt vmcnt(9)\n\t"
        "v_mfma_f32_32x32x16_bf16 v[2:17], v[42:45], v[70:73], v[2:17]\n\tds_read_b128 v[42:45], v106 offset:17280\n\ts_nop 0\n\t"
        "v_mfma_f32_32x32x16_bf16 v[18:33], v[46:49], v[70:73], v[18:33]\n\tds_read_b128 v[46:49], v107 offset:17280\n\t"
        "global_load_dwordx4 v[70:73], v108, s[20:21] offset:3072 sc1\n\ts_waitcnt vmcnt(9)\n\t"
        "v_mfma_f32_32x32x16_bf16 v[2:17], v[50:53], v[74:77], v[2:17]\n\tds_read_b128 v[50:53], v106 offset:19200\n\ts_waitcnt lgkmcnt(7)\n\t"
        "v_mfma_f32_32x32x16_bf16 v[18:33], v[54:57], v[74:77], v[18:33]\n\tds_read_b128 v[54:57], v107 offset:19200\n\ts_add_u32 s20, s20, 8192\n\t"
        "s_addc_u32 s21, s21, 0\n\tglobal_load_dwordx4 v[74:77], v108, s[20:21] offset:-4096 sc1\n\ts_waitcnt vmcnt(9) lgkmcnt(7)\n\t"
        "v_mfma_f32_32x32x16_bf16 v[2:17], v[58:61], v[78:81], v[2:17]\n\tds_read_b128 v[58:61], v106 offset:21120\n\ts_waitcnt lgkmcnt(7)\n\t"
        "v_mfma_f32_32x32x16_bf16 v[18:33], v[62:65], v[78:81], v[18:33]\n\tds_read_b128 v[62:65], v107 offset:21120\n\t"
        "global_load_dwordx4 v[78:81], v108, s[20:21] offset:-3072 sc1\n\ts_waitcnt vmcnt(9) lgkmcnt(7)\n\t"
        "v_mfma_f32_32x32x16_bf16 v[2:17], v[34:37], v[82:85], v[2:17]\n\tds_read_b128 v[34:37], v106 offset:23040\n\ts_waitcnt lgkmcnt(7)\n\t"
        "v_mfma_f32_32x32x16_bf16 v[18:33], v[38:41], v[82:85], v[18:33]\n\tds_read_b128 v[38:41], v107 offset:23040\n\t"
        "global_load_dwordx4 v[82:85], v108, s[20:21] offset:-2048 sc1\n\ts_waitcnt vmcnt(9) lgkmcnt(7)\n\t"
        "v_mfma_f32_32x32x16_bf16 v[2:17], v[42:45], v[86:89], v[2:17]\n\tds_read_b128 v[42:45], v106 offset:24960\n\ts_waitcnt lgkmcnt(7)\n\t"
        "v_mfma_f32_32x32x16_bf16 v[18:33], v[46:49], v[86:89], v[18:33]\n\tds_read_b128 v[46:49], v107 offset:24960\n\t"
        "global_load_dwordx4 v[86:89], v108, s[20:21] offset:-1024 sc1\n\ts_waitcnt vmcnt(9) lgkmcnt(7)\n\t"
        "v_mfma_f32_32x32x16_bf16 v[2:17], v[50:53], v[90:93], v[2:17]\n\tds_read_b128 v[50:53], v106 offset:26880\n\tds_read_b32 v110, v109\n\t"
        "s_add_u32 s23, %[wb], 10\n\ts_waitcnt lgkmcnt(8)\n\tv_mfma_f32_32x32x16_bf16 v[18:33], v[54:57], v[90:93], v[18:33]\n\t"
        "ds_read_b128 v[54:57], v107 offset:26880\n\tglobal_load_dwordx4 v[90:93], v108, s[20:21] offset:0 sc1\n\ts_waitcnt vmcnt(9) lgkmcnt(8)\n\t"
        "v_mfma_f32_32x32x16_bf16 v[2:17], v[58:61], v[94:97], v[2:17]\n\tds_read_b128 v[58:61], v106 offset:28800\n\ts_waitcnt lgkmcnt(8)\n\t"
        "v_mfma_f32_32x32x16_bf16 v[18:33], v[62:65], v[94:97], v[18:33]\n\tds_read_b128 v[62:65], v107 offset:28800\n\t"
        "global_load_dwordx4 v[94:97], v108, s[20:21] offset:1024 sc1\n\ts_waitcnt vmcnt(9) lgkmcnt(3)\n\tv_cmp_gt_u32 vcc, s23, v110\n\t"
        "s_cbranch_vccnz 25f\n\t15:\n\tv_mfma_f32_32x32x16_bf16 v[2:17], v[34:37], v[98:101], v[2:17]\n\tds_read_b128 v[34:37], v106 offset:30720\n\t"
        "s_nop 0\n\tv_mfma_f32_32x32x16_bf16 v[18:33], v[38:41], v[98:101], v[18:33]\n\tds_read_b128 v[38:41], v107 offset:30720\n\t"
        "global_load_dwordx4 v[98:101], v108, s[20:21] offset:2048 sc1\n\ts_waitcnt vmcnt(9)\n\t"
        "v_mfma_f32_32x32x16_bf16 v[2:17], v[42:45], v[102:105], v[2:17]\n\tds_read_b128 v[42:45], v106 offset:32640\n\ts_nop 0\n\t"
        "v_mfma_f32_32x32x16_bf16 v[18:33], v[46:49], v[102:105], v[18:33]\n\tds_read_b128 v[46:49], v107 offset:32640\n\t"
        "global_load_dwordx4 v[102:105], v108, s[20:21] offset:3072 sc1\n\ts_waitcnt vmcnt(9)\n\t"
        "v_mfma_f32_32x32x16_bf16 v[2:17], v[50:53], v[66:69], v[2:17]\n\tds_read_b128 v[50:53], v106 offset:34560\n\ts_waitcnt lgkmcnt(7)\n\t"
        "v_mfma_f32_32x32x16_bf16 v[18:33], v[54:57], v[66:69], v[18:33]\n\tds_read_b128 v[54:57], v107 offset:34560\n\ts_waitcnt vmcnt(8) lgkmcnt(7)\n\t"
        "v_mfma_f32_32x32x16_bf16 v[2:17], v[58:61], v[70:73], v[2:17]\n\tds_read_b128 v[58:61], v106 offset:36480\n\ts_waitcnt lgkmcnt(7)\n\t"
        "v_mfma_f32_32x32x16_bf16 v[18:33], v[62:65], v[70:73], v[18:33]\n\tds_read_b128 v[62:65], v107 offset:36480\n\ts_waitcnt vmcnt(7) lgkmcnt(7)\n\t"
        "v_mfma_f32_32x32x16_bf16 v[2:17], v[34:37], v[74:77], v[2:17]\n\tds_read_b128 v[34:37], v106 offset:38400\n\ts_waitcnt lgkmcnt(7)\n\t"
        "v_mfma_f32_32x32x16_bf16 v[18:33], v[38:41], v[74:77], v[18:33]\n\tds_read_b128 v[38:41], v107 offset:38400\n\ts_waitcnt vmcnt(6) lgkmcnt(7)\n\t"
        "v_mfma_f32_32x32x16_bf16 v[2:17], v[42:45], v[78:81], v[2:17]\n\tds_read_b128 v[42:45], v106 offset:40320\n\ts_add_u32 s22, %[wb], 2\n\t"
        "v_mov_b32 v120, %[qpa]\n\tv_mov_b32 v121, s22\n\tds_write_b32 v120, v121\n\ts_waitcnt lgkmcnt(8)\n\t"
        "v_mfma_f32_32x32x16_bf16 v[18:33], v[46:49], v[78:81], v[18:33]\n\tds_read_b128 v[46:49], v107 offset:40320\n\ts_waitcnt vmcnt(5) lgkmcnt(8)\n\t"
        "v_mfma_f32_32x32x16_bf16 v[2:17], v[50:53], v[82:85], v[2:17]\n\tds_read_b128 v[50:53], v106 offset:42240\n\tds_read_b32 v110, v109 offset:16\n\t"
        "s_waitcnt lgkmcnt(9)\n\tv_mfma_f32_32x32x16_bf16 v[18:33], v[54:57], v[82:85], v[18:33]\n\tds_read_b128 v[54:57], v107 offset:42240\n\t"
        "s_waitcnt vmcnt(4) lgkmcnt(9)\n\tv_mfma_f32_32x32x16_bf16 v[2:17], v[58:61], v[86:89], v[2:17]\n\tds_read_b128 v[58:61], v106 offset:44160\n\t"
        "s_waitcnt lgkmcnt(9)\n\tv_mfma_f32_32x32x16_bf16 v[18:33], v[62:65], v[86:89], v[18:33]\n\tds_read_b128 v[62:65], v107 offset:44160\n\t"
        "s_waitcnt vmcnt(3) lgkmcnt(3)\n\tv_cmp_gt_u32 vcc, %[qv], v110\n\ts_cbranch_vccnz 24f\n\t14:\n\t"
        "v_mfma_f32_32x32x16_bf16 v[2:17], v[34:37], v[90:93], v[2:17]\n\tds_read_b64 v[66:67], %[aga] offset:0\n\tds_read_b64 v[68:69], %[aga] offset:512\n\t"
        "s_nop 0\n\tv_mfma_f32_32x32x16_bf16 v[18:33], v[38:41], v[90:93], v[18:33]\n\tv_mov_b32 v80, 0x3f80\n\tv_mov_b32 v81, 0\n\t"
        "v_add_u32 v108, 98304, %[ga]\n\ts_waitcnt vmcnt(2)\n\tv_mfma_f32_32x32x16_bf16 v[2:17], v[42:45], v[94:97], v[2:17]\n\ts_nop 0\n\t"
        "v_mfma_f32_32x32x16_bf16 v[18:33], v[46:49], v[94:97], v[18:33]\n\ts_waitcnt lgkmcnt(1)\n\t"
        "v_mfma_f32_32x32x8_bf16 v[2:17], v[66:67], v[112:113], v[2:17]\n\ts_waitcnt vmcnt(1)\n\t"
        "v_mfma_f32_32x32x16_bf16 v[2:17], v[50:53], v[98:101], v[2:17]\n\ts_waitcnt lgkmcnt(0)\n\t"
        "v_mfma_f32_32x32x8_bf16 v[18:33], v[68:69], v[112:113], v[18:33]\n\ts_nop 0\n\tv_mfma_f32_32x32x16_bf16 v[18:33], v[54:57], v[98:101], v[18:33]\n\t"
        "s_waitcnt vmcnt(0)\n\tv_mfma_f32_32x32x16_bf16 v[2:17], v[58:61], v[102:105], v[2:17]\n\ts_nop 0\n\t"
        "v_mfma_f32_32x32x16_bf16 v[18:33], v[62:65], v[102:105], v[18:33]\n\ts_nop 9\n\tv_mul_f32 v2, v114, v2\n\tv_mul_f32 v3, v114, v3\n\t"
        "v_cvt_pk_bf16_f32 v70, v2, v3\n\tv_mul_f32 v4, v114, v4\n\tv_mul_f32 v5, v114, v5\n\tv_cvt_pk_bf16_f32 v71, v4, v5\n\tv_mul_f32 v6, v114, v6\n\t"
        "v_mul_f32 v7, v114, v7\n\tv_cvt_pk_bf16_f32 v72, v6, v7\n\tv_mul_f32 v8, v114, v8\n\tv_mul_f32 v9, v114, v9\n\tv_cvt_pk_bf16_f32 v73, v8, v9\n\t"
        "global_store_dwordx4 %[ga], v[70:73], %[kvd] sc1\n\tv_mul_f32 v10, v114, v10\n\tv_mul_f32 v11, v114, v11\n\tv_cvt_pk_bf16_f32 v74, v10, v11\n\t"
        "v_mul_f32 v12, v114, v12\n\tv_mul_f32 v13, v114, v13\n\tv_cvt_pk_bf16_f32 v75, v12, v13\n\tv_mul_f32 v14, v114, v14\n\tv_mul_f32 v15, v114, v15\n\t"
        "v_cvt_pk_bf16_f32 v76, v14, v15\n\tv_mul_f32 v16, v114, v16\n\tv_mul_f32 v17, v114, v17\n\tv_cvt_pk_bf16_f32 v77, v16, v17\n\t"
        "global_store_dwordx4 v108, v[74:77], %[kvd] sc1\n\tv_mul_f32 v26, v114, v26\n\tv_mul_f32 v27, v114, v27\n\tv_cvt_pk_bf16_f32 v78, v26, v27\n\t"
        "v_mul_f32 v28, v114, v28\n\tv_mul_f32 v29, v114, v29\n\tv_cvt_pk_bf16_f32 v79, v28, v29\n\tglobal_store_dwordx4 %[gb], v[78:81], %[kvd] sc1\n\t"
        "v_mul_f32 v30, v115, v30\n\tv_mul_f32 v31, v115, v31\n\tv_cvt_pk_bf16_f32 v82, v30, v31\n\tv_mul_f32 v32, v115, v32\n\tv_mul_f32 v33, v115, v33\n\t"
        "v_cvt_pk_bf16_f32 v83, v32, v33\n\ts_mov_b32 exec_lo, 0\n\tv_mov_b32 v82, 0\n\tv_mov_b32 v83, 0\n\ts_mov_b32 exec_lo, -1\n\t"
        "v_mul_f32 v18, v115, v18\n\tv_mul_f32 v19, v115, v19\n\tv_cvt_pk_bf16_f32 v86, v18, v19\n\tv_mul_f32 v20, v115, v20\n\tv_mul_f32 v21, v115, v21\n\t"
        "v_cvt_pk_bf16_f32 v87, v20, v21\n\tv_mul_f32 v22, v115, v22\n\tv_mul_f32 v23, v115, v23\n\tv_cvt_pk_bf16_f32 v88, v22, v23\n\t"
        "v_mul_f32 v24, v115, v24\n\tv_mul_f32 v25, v115, v25\n\tv_cvt_pk_bf16_f32 v89, v24, v25\n\tds_write_b128 %[da], v[70:73]\n\t"
        "ds_write_b128 %[da], v[74:77] offset:24576\n\tv_mfma_f32_32x32x16_bf16 v[124:139], v[70:73], v[86:89], v[124:139]\n\t"
        "v_mfma_f32_32x32x8_bf16 v[124:139], v[78:79], v[82:83], v[124:139]\n\tds_write_b128 %[db], v[78:81]\n\tv_mov_b32 v111, %[qda]\n\t"
        "v_mov_b32 v110, %[qdv]\n\tds_write_b32 v111, v110\n\ts_nop 7\n\ts_branch 4f\n\t20:\n\ts_cmp_eq_u32 %[bud], 0\n\ts_cbranch_scc1 10b\n\t"
        "s_sub_u32 %[bud], %[bud], 1\n\ts_sleep 1\n\tds_read_b32 v110, v109\n\ts_waitcnt lgkmcnt(0)\n\tv_cmp_gt_u32 vcc, s23, v110\n\ts_cbranch_vccnz 20b\n\t"
        "s_branch 10b\n\t21:\n\ts_cmp_eq_u32 %[bud], 0\n\ts_cbranch_scc1 11b\n\ts_sub_u32 %[bud], %[bud], 1\n\ts_sleep 1\n\tds_read_b32 v110, v109\n\t"
        "s_waitcnt lgkmcnt(0)\n\tv_cmp_gt_u32 vcc, s23, v110\n\ts_cbranch_vccnz 21b\n\ts_branch 11b\n\t22:\n\ts_cmp_eq_u32 %[bud], 0\n\ts_cbranch_scc1 12b\n\t"
        "s_sub_u32 %[bud], %[bud], 1\n\ts_sleep 1\n\tds_read_b32 v110, v109\n\ts_waitcnt lgkmcnt(0)\n\tv_cmp_gt_u32 vcc, s23, v110\n\ts_cbranch_vccnz 22b\n\t"
        "s_branch 12b\n\t23:\n\ts_cmp_eq_u32 %[bud], 0\n\ts_cbranch_scc1 13b\n\ts_sub_u32 %[bud], %[bud], 1\n\ts_sleep 1\n\tds_read_b32 v110, v109\n\t"
        "s_waitcnt lgkmcnt(0)\n\tv_cmp_gt_u32 vcc, s23, v110\n\ts_cbranch_vccnz 23b\n\ts_branch 13b\n\t25:\n\ts_cmp_eq_u32 %[bud], 0\n\ts_cbranch_scc1 15b\n\t"
        "s_sub_u32 %[bud], %[bud], 1\n\ts_sleep 1\n\tds_read_b32 v110, v109\n\ts_waitcnt lgkmcnt(0)\n\tv_cmp_gt_u32 vcc, s23, v110\n\ts_cbranch_vccnz 25b\n\t"
        "s_branch 15b\n\t24:\n\ts_cmp_eq_u32 %[bud], 0\n\ts_cbranch_scc1 14b\n\ts_sub_u32 %[bud], %[bud], 1\n\ts_sleep 1\n\t"
        "ds_read_b32 v110, v109 offset:16\n\ts_waitcnt lgkmcnt(0)\n\tv_cmp_gt_u32 vcc, %[qv], v110\n\ts_cbranch_vccnz 24b\n\ts_branch 14b\n\t4:\n\t"
        : "=&{v[86:89]}"(qm), "=&{v[82:83]}"(qt), "+{v[124:139]}"(s0), "+{v106}"(wa), "+{v107}"(wta), "+{v111}"(soff), "+{v108}"(xoff), "+{s[20:21]}"(xsrc), [bud] "+&s"(budget)
        : "{v109}"(rdya), [da] "v"(da), [db] "v"(db), [aga] "v"(aga), [ssrc] "s"(ssrc), [ninvd] "s"(-1.f / DM),
          [eps] "s"(1e-5f), [qsc] "s"(QSCALE), [wb] "s"(wb), [qv] "s"(qv), [qda] "s"(qda), [qdv] "s"(qdv), [qpa] "s"(qpa), [kvd] "s"(kvd), [ga] "v"(ga), [gb] "v"(gb)
        : "v2", "v3", "v4", "v5", "v6", "v7", "v8", "v9", "v10", "v11", "v12", "v13", "v14", "v15", "v16", "v17", "v18", "v19", "v20", "v21", "v22",
          "v23", "v24", "v25", "v26", "v27", "v28", "v29", "v30", "v31", "v32", "v33", "v34", "v35", "v36", "v37", "v38", "v39", "v40", "v41", "v42",
          "v43", "v44", "v45", "v46", "v47", "v48", "v49", "v50", "v51", "v52", "v53", "v54", "v55", "v56", "v57", "v58", "v59", "v60", "v61", "v62",
          "v63", "v64", "v65", "v66", "v67", "v68", "v69", "v70", "v71", "v72", "v73", "v74", "v75", "v76", "v77", "v78", "v79", "v80", "v81", "v84",
          "v85", "v90", "v91", "v92", "v93", "v94", "v95", "v96", "v97", "v98", "v99", "v100", "v101", "v102", "v103", "v104", "v105", "v110", "v112",
          "v113", "v114", "v115", "v116", "v117", "v118", "v119", "v120", "v121", "v122", "v123",
          "s22", "s23", "vcc", "scc", "memory");
}

DI void wload0(unsigned wait, f32x32& d, const char* src, unsigned voff, unsigned lastoff) {
    asm volatile(
        "s_cmp_eq_u32 %[wait], 0\n\ts_cbranch_scc1 1f\n\ts_nop 2\n\tglobal_load_dwordx4 v[8:11], %[voff], s[20:21] sc1\n\ts_waitcnt vmcnt(2)\n\t"
        "s_add_u32 s20, s20, 4096\n\ts_addc_u32 s21, s21, 0\n\tglobal_load_dwordx4 v[12:15], %[voff], s[20:21] sc1\n\ts_waitcnt vmcnt(2)\n\t"
        "s_add_u32 s20, s20, 4096\n\ts_addc_u32 s21, s21, 0\n\tglobal_load_dwordx4 v[16:19], %[voff], s[20:21] sc1\n\ts_waitcnt vmcnt(2)\n\t"
        "s_add_u32 s20, s20, 4096\n\ts_addc_u32 s21, s21, 0\n\tglobal_load_dwordx4 v[20:23], %[voff], s[20:21] sc1\n\ts_waitcnt vmcnt(2)\n\t"
        "s_add_u32 s20, s20, 4096\n\ts_addc_u32 s21, s21, 0\n\tglobal_load_dwordx4 v[24:27], %[voff], s[20:21] sc1\n\ts_waitcnt vmcnt(2)\n\t"
        "s_add_u32 s20, s20, 4096\n\ts_addc_u32 s21, s21, 0\n\tglobal_load_dwordx4 v[28:31], %[voff], s[20:21] sc1\n\ts_waitcnt vmcnt(2)\n\t"
        "s_add_u32 s20, s20, 4096\n\ts_addc_u32 s21, s21, 0\n\tglobal_load_dwordx4 v[32:35], %[voff], s[20:21] sc1\n\ts_waitcnt vmcnt(2)\n\t"
        "s_add_u32 s20, s20, 4096\n\ts_addc_u32 s21, s21, 0\n\tglobal_load_dwordx4 v[36:39], %[voff], s[20:21] sc1\n\ts_waitcnt vmcnt(2)\n\t"
        "s_cmp_eq_u32 %[wait], 1\n\ts_cbranch_scc1 1f\n\ts_waitcnt vmcnt(0)\n\t1:\n\t"
        : "=&{v[8:39]}"(d), "+{s[20:21]}"(src) : [voff] "v"(voff), [lastoff] "v"(lastoff), [wait] "s"(wait) : "scc", "memory");
}
DI void wreload0(unsigned wait, f32x32& d, const char* src, unsigned voff, unsigned lastoff) {
    asm volatile(
        "s_cmp_eq_u32 %[wait], 0\n\ts_cbranch_scc1 1f\n\ts_nop 2\n\tglobal_load_dwordx4 v[8:11], %[voff], s[20:21] sc1\n\ts_waitcnt vmcnt(2)\n\t"
        "s_add_u32 s20, s20, 4096\n\ts_addc_u32 s21, s21, 0\n\tglobal_load_dwordx4 v[12:15], %[voff], s[20:21] sc1\n\ts_waitcnt vmcnt(2)\n\t"
        "s_add_u32 s20, s20, 4096\n\ts_addc_u32 s21, s21, 0\n\tglobal_load_dwordx4 v[16:19], %[voff], s[20:21] sc1\n\ts_waitcnt vmcnt(2)\n\t"
        "s_add_u32 s20, s20, 4096\n\ts_addc_u32 s21, s21, 0\n\tglobal_load_dwordx4 v[20:23], %[voff], s[20:21] sc1\n\ts_waitcnt vmcnt(2)\n\t"
        "s_add_u32 s20, s20, 4096\n\ts_addc_u32 s21, s21, 0\n\tglobal_load_dwordx4 v[24:27], %[voff], s[20:21] sc1\n\ts_waitcnt vmcnt(2)\n\t"
        "s_add_u32 s20, s20, 4096\n\ts_addc_u32 s21, s21, 0\n\tglobal_load_dwordx4 v[28:31], %[voff], s[20:21] sc1\n\ts_waitcnt vmcnt(2)\n\t"
        "s_add_u32 s20, s20, 4096\n\ts_addc_u32 s21, s21, 0\n\tglobal_load_dwordx4 v[32:35], %[voff], s[20:21] sc1\n\ts_waitcnt vmcnt(2)\n\t"
        "s_add_u32 s20, s20, 4096\n\ts_addc_u32 s21, s21, 0\n\tglobal_load_dwordx4 v[36:39], %[voff], s[20:21] sc1\n\ts_waitcnt vmcnt(2)\n\t"
        "s_cmp_eq_u32 %[wait], 1\n\ts_cbranch_scc1 1f\n\ts_waitcnt vmcnt(0)\n\t1:\n\t"
        : "+{v[8:39]}"(d), "+{s[20:21]}"(src) : [voff] "v"(voff), [lastoff] "v"(lastoff), [wait] "s"(wait) : "scc", "memory");
}
DI void wdump0(f32x32 d, unsigned a, unsigned lasta) {
    asm volatile(
        "ds_write_b128 %[a], v[8:11] offset:0\n\tds_write_b128 %[a], v[12:15] offset:4096\n\tds_write_b128 %[a], v[16:19] offset:8192\n\t"
        "ds_write_b128 %[a], v[20:23] offset:12288\n\tds_write_b128 %[a], v[24:27] offset:16384\n\tds_write_b128 %[a], v[28:31] offset:20480\n\t"
        "ds_write_b128 %[a], v[32:35] offset:24576\n\tds_write_b128 %[a], v[36:39] offset:28672\n\t"
        :: "{v[8:39]}"(d), [a] "v"(a), [lasta] "v"(lasta) : "memory");
}
DI void wload1(unsigned wait, f32x32& d, const char* src, unsigned voff, unsigned lastoff) {
    asm volatile(
        "s_cmp_eq_u32 %[wait], 0\n\ts_cbranch_scc1 1f\n\ts_nop 2\n\tglobal_load_dwordx4 v[40:43], %[voff], s[20:21] sc1\n\ts_waitcnt vmcnt(2)\n\t"
        "s_add_u32 s20, s20, 4096\n\ts_addc_u32 s21, s21, 0\n\tglobal_load_dwordx4 v[44:47], %[voff], s[20:21] sc1\n\ts_waitcnt vmcnt(2)\n\t"
        "s_add_u32 s20, s20, 4096\n\ts_addc_u32 s21, s21, 0\n\tglobal_load_dwordx4 v[48:51], %[voff], s[20:21] sc1\n\ts_waitcnt vmcnt(2)\n\t"
        "s_add_u32 s20, s20, 4096\n\ts_addc_u32 s21, s21, 0\n\tglobal_load_dwordx4 v[52:55], %[voff], s[20:21] sc1\n\ts_waitcnt vmcnt(2)\n\t"
        "s_add_u32 s20, s20, 4096\n\ts_addc_u32 s21, s21, 0\n\tglobal_load_dwordx4 v[56:59], %[voff], s[20:21] sc1\n\ts_waitcnt vmcnt(2)\n\t"
        "s_add_u32 s20, s20, 4096\n\ts_addc_u32 s21, s21, 0\n\tglobal_load_dwordx4 v[60:63], %[voff], s[20:21] sc1\n\ts_waitcnt vmcnt(2)\n\t"
        "s_add_u32 s20, s20, 4096\n\ts_addc_u32 s21, s21, 0\n\tglobal_load_dwordx4 v[64:67], %[voff], s[20:21] sc1\n\ts_waitcnt vmcnt(2)\n\t"
        "s_add_u32 s20, s20, 4096\n\ts_addc_u32 s21, s21, 0\n\tglobal_load_dwordx4 v[68:71], %[voff], s[20:21] sc1\n\ts_waitcnt vmcnt(2)\n\t"
        "s_cmp_eq_u32 %[wait], 1\n\ts_cbranch_scc1 1f\n\ts_waitcnt vmcnt(0)\n\t1:\n\t"
        : "=&{v[40:71]}"(d), "+{s[20:21]}"(src) : [voff] "v"(voff), [lastoff] "v"(lastoff), [wait] "s"(wait) : "scc", "memory");
}
DI void wreload1(unsigned wait, f32x32& d, const char* src, unsigned voff, unsigned lastoff) {
    asm volatile(
        "s_cmp_eq_u32 %[wait], 0\n\ts_cbranch_scc1 1f\n\ts_nop 2\n\tglobal_load_dwordx4 v[40:43], %[voff], s[20:21] sc1\n\ts_waitcnt vmcnt(2)\n\t"
        "s_add_u32 s20, s20, 4096\n\ts_addc_u32 s21, s21, 0\n\tglobal_load_dwordx4 v[44:47], %[voff], s[20:21] sc1\n\ts_waitcnt vmcnt(2)\n\t"
        "s_add_u32 s20, s20, 4096\n\ts_addc_u32 s21, s21, 0\n\tglobal_load_dwordx4 v[48:51], %[voff], s[20:21] sc1\n\ts_waitcnt vmcnt(2)\n\t"
        "s_add_u32 s20, s20, 4096\n\ts_addc_u32 s21, s21, 0\n\tglobal_load_dwordx4 v[52:55], %[voff], s[20:21] sc1\n\ts_waitcnt vmcnt(2)\n\t"
        "s_add_u32 s20, s20, 4096\n\ts_addc_u32 s21, s21, 0\n\tglobal_load_dwordx4 v[56:59], %[voff], s[20:21] sc1\n\ts_waitcnt vmcnt(2)\n\t"
        "s_add_u32 s20, s20, 4096\n\ts_addc_u32 s21, s21, 0\n\tglobal_load_dwordx4 v[60:63], %[voff], s[20:21] sc1\n\ts_waitcnt vmcnt(2)\n\t"
        "s_add_u32 s20, s20, 4096\n\ts_addc_u32 s21, s21, 0\n\tglobal_load_dwordx4 v[64:67], %[voff], s[20:21] sc1\n\ts_waitcnt vmcnt(2)\n\t"
        "s_add_u32 s20, s20, 4096\n\ts_addc_u32 s21, s21, 0\n\tglobal_load_dwordx4 v[68:71], %[voff], s[20:21] sc1\n\ts_waitcnt vmcnt(2)\n\t"
        "s_cmp_eq_u32 %[wait], 1\n\ts_cbranch_scc1 1f\n\ts_waitcnt vmcnt(0)\n\t1:\n\t"
        : "+{v[40:71]}"(d), "+{s[20:21]}"(src) : [voff] "v"(voff), [lastoff] "v"(lastoff), [wait] "s"(wait) : "scc", "memory");
}
DI void wdump1(f32x32 d, unsigned a, unsigned lasta) {
    asm volatile(
        "ds_write_b128 %[a], v[40:43] offset:0\n\tds_write_b128 %[a], v[44:47] offset:4096\n\tds_write_b128 %[a], v[48:51] offset:8192\n\t"
        "ds_write_b128 %[a], v[52:55] offset:12288\n\tds_write_b128 %[a], v[56:59] offset:16384\n\tds_write_b128 %[a], v[60:63] offset:20480\n\t"
        "ds_write_b128 %[a], v[64:67] offset:24576\n\tds_write_b128 %[a], v[68:71] offset:28672\n\t"
        :: "{v[40:71]}"(d), [a] "v"(a), [lasta] "v"(lasta) : "memory");
}
DI void wload2(unsigned wait, f32x16& d, const char* src, unsigned voff, unsigned lastoff) {
    asm volatile(
        "s_cmp_eq_u32 %[wait], 0\n\ts_cbranch_scc1 1f\n\ts_nop 2\n\tglobal_load_dwordx4 v[72:75], %[voff], s[20:21] sc1\n\ts_waitcnt vmcnt(2)\n\t"
        "s_add_u32 s20, s20, 4096\n\ts_addc_u32 s21, s21, 0\n\tglobal_load_dwordx4 v[76:79], %[voff], s[20:21] sc1\n\ts_waitcnt vmcnt(2)\n\t"
        "global_load_dwordx4 v[80:83], %[lastoff], s[20:21] sc1\n\ts_waitcnt vmcnt(2)\n\ts_cmp_eq_u32 %[wait], 1\n\ts_cbranch_scc1 1f\n\ts_waitcnt vmcnt(0)\n\t"
        "1:\n\t"
        : "=&{v[72:87]}"(d), "+{s[20:21]}"(src) : [voff] "v"(voff), [lastoff] "v"(lastoff), [wait] "s"(wait) : "scc", "memory");
}
DI void wreload2(unsigned wait, f32x16& d, const char* src, unsigned voff, unsigned lastoff) {
    asm volatile(
        "s_cmp_eq_u32 %[wait], 0\n\ts_cbranch_scc1 1f\n\ts_nop 2\n\tglobal_load_dwordx4 v[72:75], %[voff], s[20:21] sc1\n\ts_waitcnt vmcnt(2)\n\t"
        "s_add_u32 s20, s20, 4096\n\ts_addc_u32 s21, s21, 0\n\tglobal_load_dwordx4 v[76:79], %[voff], s[20:21] sc1\n\ts_waitcnt vmcnt(2)\n\t"
        "global_load_dwordx4 v[80:83], %[lastoff], s[20:21] sc1\n\ts_waitcnt vmcnt(2)\n\ts_cmp_eq_u32 %[wait], 1\n\ts_cbranch_scc1 1f\n\ts_waitcnt vmcnt(0)\n\t"
        "1:\n\t"
        : "+{v[72:87]}"(d), "+{s[20:21]}"(src) : [voff] "v"(voff), [lastoff] "v"(lastoff), [wait] "s"(wait) : "scc", "memory");
}
DI void wdump2(f32x16 d, unsigned a, unsigned lasta) {
    asm volatile(
        "ds_write_b128 %[a], v[72:75] offset:0\n\tds_write_b128 %[a], v[76:79] offset:4096\n\tds_write_b128 %[lasta], v[80:83] offset:8192\n\t"
        :: "{v[72:87]}"(d), [a] "v"(a), [lasta] "v"(lasta) : "memory");
}

DI void kv_stream(unsigned& n, unsigned nend, unsigned& seen, unsigned& budget, unsigned rb, const char* mb, const char* ksrc, unsigned kdst,
                  unsigned pb, unsigned rbase, u64 tag, unsigned g0, unsigned g1, unsigned g2, unsigned moff, unsigned prog, unsigned rdy) {
    unsigned t; u64 x;
    asm volatile(
        "s_nop 4\n\t1:\n\ts_mov_b32 s22, 0\n\ts_add_u32 s26, %[n], 1\n\ts_sub_u32 s20, s26, 4\n\ts_cmp_ge_u32 s20, 26\n\ts_cselect_b32 s21, 2, 0\n\t"
        "s_add_u32 s20, s20, s21\n\ts_lshr_b32 s21, s20, 3\n\ts_lshl_b32 s21, s21, 1\n\ts_bfe_u32 s23, s20, 0x10001\n\ts_add_u32 s21, s21, s23\n\t"
        "s_add_u32 s21, s21, %[rb]\n\ts_add_u32 s21, s21, 1\n\ts_and_b32 s21, s21, 7\n\ts_lshl_b32 s21, s21, 1\n\ts_bfe_u32 s23, s20, 0x10002\n\t"
        "s_xor_b32 s23, s23, 1\n\ts_or_b32 s21, s21, s23\n\ts_bitset1_b32 s22, s21\n\ts_lshl_b32 s21, s21, 1\n\ts_and_b32 s20, s20, 1\n\t"
        "s_or_b32 s26, s21, s20\n\ts_max_u32 s27, %[n], 4\n\ts_sub_u32 s20, s27, 4\n\ts_cmp_ge_u32 s20, 26\n\ts_cselect_b32 s21, 2, 0\n\t"
        "s_add_u32 s20, s20, s21\n\ts_lshr_b32 s21, s20, 3\n\ts_lshl_b32 s21, s21, 1\n\ts_bfe_u32 s23, s20, 0x10001\n\ts_add_u32 s21, s21, s23\n\t"
        "s_add_u32 s21, s21, %[rb]\n\ts_add_u32 s21, s21, 1\n\ts_and_b32 s21, s21, 7\n\ts_lshl_b32 s21, s21, 1\n\ts_bfe_u32 s23, s20, 0x10002\n\t"
        "s_xor_b32 s23, s23, 1\n\ts_or_b32 s21, s21, s23\n\ts_bitset1_b32 s22, s21\n\ts_lshl_b32 s21, s21, 1\n\ts_and_b32 s20, s20, 1\n\t"
        "s_or_b32 s27, s21, s20\n\t2:\n\ts_andn2_b32 s21, s22, %[seen]\n\ts_cbranch_scc0 3f\n\ts_cmp_eq_u32 %[bud], 0\n\ts_cbranch_scc1 3f\n\t"
        "s_sub_u32 %[bud], %[bud], 1\n\ts_andn2_b32 exec_lo, 0xffff, %[seen]\n\ts_mov_b32 exec_hi, 0\n\t"
        "global_atomic_or_x2 %[x], %[moff], %[z], %[mb] sc0 sc1\n\ts_waitcnt vmcnt(0)\n\tv_cmp_eq_u64 vcc, %[tag], %[x]\n\ts_mov_b64 exec, -1\n\t"
        "s_or_b32 %[seen], %[seen], vcc_lo\n\ts_branch 2b\n\t3:\n\ts_cmp_lt_u32 %[n], 7\n\ts_cbranch_scc1 5f\n\ts_sub_u32 s22, %[n], 6\n\t"
        "s_add_u32 s22, s22, %[pb]\n\t4:\n\tds_read_b32 %[t], %[prog]\n\ts_waitcnt lgkmcnt(0)\n\tv_cmp_gt_u32 vcc, s22, %[t]\n\ts_cbranch_vccz 5f\n\t"
        "s_cmp_eq_u32 %[bud], 0\n\ts_cbranch_scc1 5f\n\ts_sub_u32 %[bud], %[bud], 1\n\ts_sleep 2\n\ts_branch 4b\n\t5:\n\ts_add_u32 s21, %[n], 1\n\t"
        "s_and_b32 s21, s21, 7\n\ts_mul_i32 s21, s21, 3072\n\ts_add_u32 m0, s21, %[kdst]\n\ts_mul_i32 s22, s26, 3072\n\ts_add_u32 s22, s24, s22\n\t"
        "s_addc_u32 s23, s25, 0\n\tglobal_load_lds_dwordx4 %[g0], s[22:23] sc1\n\tglobal_load_lds_dwordx4 %[g1], s[22:23] offset:1024 sc1\n\t"
        "global_load_lds_dwordx4 %[g2], s[22:23] offset:2048 sc1\n\ts_cmp_eq_u32 %[n], 3\n\ts_cbranch_scc1 6f\n\ts_and_b32 s21, %[n], 7\n\t"
        "s_mul_i32 s21, s21, 3072\n\ts_add_u32 s21, s21, 24576\n\ts_add_u32 m0, s21, %[kdst]\n\ts_mul_i32 s22, s27, 3072\n\ts_add_u32 s22, s22, 98304\n\t"
        "s_add_u32 s22, s24, s22\n\ts_addc_u32 s23, s25, 0\n\tglobal_load_lds_dwordx4 %[g0], s[22:23] sc1\n\t"
        "global_load_lds_dwordx4 %[g1], s[22:23] offset:1024 sc1\n\tglobal_load_lds_dwordx4 %[g2], s[22:23] offset:2048 sc1\n\t6:\n\ts_or_b32 s21, %[n], 1\n\t"
        "s_add_u32 s21, %[rbase], s21\n\tv_mov_b32 %[t], s21\n\ts_add_u32 %[n], %[n], 4\n\ts_waitcnt vmcnt(0)\n\tds_write_b32 %[rdy], %[t]\n\t"
        "s_cmp_lt_u32 %[n], %[nend]\n\ts_cbranch_scc1 1b\n\t"
        : [n] "+&s"(n), [seen] "+&s"(seen), [bud] "+&s"(budget), [t] "=&v"(t), [x] "=&v"(x)
        : [nend] "s"(nend), [rb] "s"(rb), [mb] "s"(mb), "{s[24:25]}"(ksrc), [kdst] "s"(kdst), [pb] "s"(pb), [rbase] "s"(rbase),
          [tag] "s"(tag), [g0] "v"(g0), [g1] "v"(g1), [g2] "v"(g2), [moff] "v"(moff), [z] "v"(0ull), [prog] "v"(prog), [rdy] "v"(rdy)
        : "s20", "s21", "s22", "s23", "s26", "s27", "vcc", "scc", "memory");
}

#define FETCH_DELAY 8
#define MAPMODE 2
#define QPRIO 1
#define LPRIO 3
#define PUBPOS 1
#define LOWFIRST 1
#define GATE 0
#define DUMPAT 1
#define DUMPSL 2
#define LPMASK 10
#define BGW 63
#define WOSPLIT 76

__global__ __launch_bounds__(768) void k_main(const char* xin, const char* wqA, const char* woA,
                                              const char* wqB, const char* woB, char* out, char* ws) {
    extern __shared__ __attribute__((aligned(16))) char lds_raw[];
    u64 entry_pc;
    asm volatile("s_getpc_b64 %0" : "=s"(entry_pc));
    if (__builtin_amdgcn_readfirstlane(threadIdx.x) >= 704) {
        asm volatile("s_mov_b32 m0, %2\n\ts_mov_b64 exec, 0xff\n\tglobal_load_lds_dword %0, %1\n\ts_mov_b64 exec, -1"
                     :: "v"((unsigned)min((int)((blockIdx.x >> 3) * 8 + (threadIdx.x & 7)) * 128, CODE_BYTES - 128)), "s"(entry_pc & ~255ull), "s"(L_SINK) : "memory");
    }
    const lds_ptr lds = (lds_ptr)lds_raw;
    const unsigned lds0 = (unsigned)(uintptr_t)lds_raw;
    int rb, hd;
    {
        const int xcd = blockIdx.x & 7, j = blockIdx.x >> 3;
        if (MAPMODE == 0) { hd = xcd * 4 + (j & 3); rb = j >> 2; }
        else if (MAPMODE == 1) { hd = (xcd & 3) * 8 + (j & 7); rb = (xcd >> 2) * 4 + (j >> 3); }
        else if (MAPMODE == 2) { hd = (xcd & 1) * 16 + (j & 15); rb = (xcd >> 1) * 2 + (j >> 4); }
        else { hd = j; rb = xcd; }
    }
    const int lane = threadIdx.x & 63, r = lane & 31, h = lane >> 5;
    int w = __builtin_amdgcn_readfirstlane(threadIdx.x >> 6);
#define bid (rb * 32 + hd)
#define FRESH_SCALARS() asm volatile("" : "+s"(rb), "+s"(hd), "+s"(w), "+s"(ws), "+s"(l))
    unsigned budget = SPIN_BUDGET;
    ST(56);
    asm volatile("" : "+s"(xin), "+s"(wqA), "+s"(woA), "+s"(wqB), "+s"(woB), "+s"(out), "+s"(ws));

    if (w < 10) {
        const bool isw = w < 5;
        const char* src = isw ? wqA + ((rb * 2 + (hd & 1)) * 5 + w) * 30720 + (hd >> 1) * 80 : xin + bid * 10240 + (w - 5) * 256;
        const unsigned mul = isw ? 137 : 256, div = isw ? 15 : 8, pitch = isw ? 3840 : 1280, skip = isw ? 1200 : 0;
        auto piece = [&](unsigned idx) { const unsigned row = (idx * mul) >> 11, c = idx - row * div; return row * pitch + ((c * 13) >> 6) * skip + c * 16; };
        set_m0(lds0 + L_RING + w * 2048);
        dma16p<0>(src, piece(lane));
        dma16p<1024>(src - (isw ? 1024 : 896), piece(min(lane + (isw ? 64 : 0), 119)));
    }
    SBAR();
    u64 tag64;
    {
        const int hp = hd >> 1, hs = hd & 1, sl = rb * 2 + hs;
        auto wq_fetch = [&](const char* wq, int ko, unsigned stg) {
            const char* src = wq + (size_t)ko * 8 * 3840 + hp * 80;
            set_m0(stg);
#pragma unroll
            for (int i = 0; i < 2; ++i) {
                const int idx = min(i * 64 + lane, 119), row = (idx * 137) >> 11, c = idx - row * 15, type = (c * 13) >> 6;
                const unsigned off = row * 3840 + type * 1200 + c * 16;
                if (i == 0) dma16p<0>(uni(src), off); else dma16p<1024>(uni(src - 1024), off);
            }
        };
        const int qg = lane >> 4, qp = lane & 3, qtype = qg < 3 ? (qg == 2 ? 0 : qg + 1) : (qp >= 2 ? 0 : qp + 1);
        unsigned sa0 = ((lane & 15) >> 2) * 240 + (qtype * 10 + (qg < 3 ? (qp & 1) * 2 + (qp >> 1) : 4)) * 8, sa1 = sa0 + (qg < 3 ? 48 : 8);
        auto wq_read = [&](unsigned stg, u32x4& v0, u32x4& v1) {
            const lds_ptr a0 = (lds_ptr)(uintptr_t)(stg + sa0), a1 = (lds_ptr)(uintptr_t)(stg + sa1);
            v0 = __builtin_bit_cast(u32x4, __builtin_shufflevector(lds_tr64(a0), lds_tr64(a0 + 960), 0, 1, 2, 3, 4, 5, 6, 7));
            v1 = __builtin_bit_cast(u32x4, __builtin_shufflevector(lds_tr64(a1), lds_tr64(a1 + 960), 0, 1, 2, 3, 4, 5, 6, 7));
            asm volatile("" : "+v"(v0), "+v"(v1));
        };
        auto wq_sums = [&](int pmat, int kol, u32x4 v0, u32x4 v1) {
            const lds_ptr c = lds + L_STG + 18432 + (pmat * 5 + kol) * 512 + lane * 4;
            { const unsigned u0 = v0[0], u1 = v0[1], u2 = v0[2], u3 = v0[3]; *(LAS float*)c = dot_ones(u3, dot_ones(u2, dot_ones(u1, dot_ones(u0, 0.f)))); }
            { const unsigned u0 = v1[0], u1 = v1[1], u2 = v1[2], u3 = v1[3]; *(LAS float*)(c + 256) = dot_ones(u3, dot_ones(u2, dot_ones(u1, dot_ones(u0, 0.f)))); }
        };
        auto wq_store = [&](int pmat, int kol, u32x4 v0, u32x4 v1) {
            char* dst = uni(ws + OFF_WQ + pmat * WQ_BYTES + (size_t)hp * 153600 + (sl * 5 + kol) * 960);
            if (lane < 60) { gst16(dst, lane * 16, v0); gst16(dst + 76800, lane * 16, v1); }
        };
        auto cs_store = [&](int mm) {
            const lds_ptr c = lds + L_STG + 18432 + mm * 2560 + lane * 8;
            const f32x2 v = (*(const LAS f32x2*)c + *(const LAS f32x2*)(c + 512)) + (*(const LAS f32x2*)(c + 1024) + *(const LAS f32x2*)(c + 1536)) + *(const LAS f32x2*)(c + 2048);
            gst8(ws + OFF_CS, (unsigned)(((mm * 16 + hp) * 16 + sl) * 512 + lane * 8), __builtin_bit_cast(u32x2, v));
        };
        if (w < 5) {
            ST(61);
            asm volatile("s_barrier\n\ts_load_dwordx2 %0, %1, 0x0" : "=s"(tag64) : "s"(ws + OFF_EPOCH) : "memory");
            asm volatile("s_waitcnt vmcnt(0)" : "+v"(sa0), "+v"(sa1) :: "memory");
            ST(62);
            u32x4 v0, v1;
            wq_read(lds0 + L_RING + w * 2048, v0, v1);
            wq_store(0, w, v0, v1);
            wq_sums(0, w, v0, v1);
            asm volatile("s_waitcnt lgkmcnt(0)" : "+s"(tag64) :: "memory");
        } else if (w < 10) {
            const int t0 = rb * 256 + hd * 8, tok = lane & 7, ch = (w - 5) * 16 + (lane >> 3);
            asm volatile("s_barrier\n\ts_load_dwordx2 %0, %1, 0x0" : "=s"(tag64) : "s"(ws + OFF_EPOCH) : "memory");
            char* xd = uni(ws + OFF_X + (size_t)(t0 >> 5) * 40960 + (t0 & 31) * 16);
            char* sd = uni(ws + OFF_ST + (t0 >> 4) * 1024 + (t0 & 15) * 8);
            const lds_ptr xs = lds + L_RING + w * 2048 + tok * 128 + (lane >> 3) * 16;
            DRAIN();
            const u32x4 v0 = *(const LAS u32x4*)xs, v1 = *(const LAS u32x4*)(xs + 1024);
            gst16(xd, ch * 512 + tok * 16, v0); gst16(xd + 4096, ch * 512 + tok * 16, v1);
            SBAR();
            float s = 0.f, q = 0.f;
#pragma unroll
            for (int e = 0; e < 4; ++e) { const unsigned a = v0[e], c = v1[e]; s = dot_ones(c, dot_ones(a, s)); q = dot_self(c, dot_self(a, q)); }
            s += __builtin_bit_cast(float, __builtin_amdgcn_update_dpp(0, __builtin_bit_cast(int, s), 0x128, 0xf, 0xf, false));
            q += __builtin_bit_cast(float, __builtin_amdgcn_update_dpp(0, __builtin_bit_cast(int, q), 0x128, 0xf, 0xf, false));
            s = half_swap_add(quarter_swap_add(s)); q = half_swap_add(quarter_swap_add(q));
            const int sslot = w - 5 + (lane >> 3) * 5;
            if (sslot < 8) gst8(sd, (unsigned)(sslot * 128 + tok * 8), lane < 8 ? (u32x2){__float_as_uint(s), __float_as_uint(q)} : (u32x2){0u, 0u});
            asm volatile("s_waitcnt lgkmcnt(0)" : "+s"(tag64) :: "memory");
        } else {
            const int m = w & 1;
            if (m == 0) {
                unsigned ml = (unsigned)(OFF_MBX + ((lane >> 2) * 256 + bid) * 512 + (lane & 3) * 128);
                ml = lane >= 32 ? (unsigned)(OFF_MBP + bid * 256 + (lane & 1) * 128) : ml;
                ml = lane >= 34 ? (unsigned)(OFF_MBW + ((lane & 1) * 256 + bid) * 128) : ml;
                ml = lane >= 36 ? (unsigned)(OFF_MBKV + (((lane & 3) * 32 + hd) * 8 + rb) * 128) : ml;
                asm volatile("s_mov_b32 exec_hi, 0xff\n\tglobal_atomic_or %0, %1, %2 sc1\n\ts_mov_b32 exec_hi, -1" :: "v"(ml), "v"(0u), "s"(ws) : "memory");
            } else {
#pragma unroll
                for (int i = 0; i < 8; ++i) *(LAS unsigned*)(lds + L_PROG + i * 256 + lane * 4) = 0u;
                *(LAS unsigned*)(lds + L_WRDY + lane * 4) = 0u;
            }
            WG_BARRIER();
            {
                unsigned t;
                asm volatile("global_load_dword %0, %1, %2\n\ts_waitcnt vmcnt(0)" : "=&v"(t) : "v"(0u), "s"(uni(xin + (size_t)bid * 10240)) : "memory");
                __builtin_amdgcn_s_sleep(FETCH_DELAY);
            }
            const int hf = hp & 1;
            const char* src = (m ? woB : woA) + (size_t)sl * 40 * 1280 + (hp >> 1) * 160;
#pragma unroll
            for (int i = 0; i < 4; ++i) {
                const int idx = min(i * 64 + lane, 199), row = (idx * 205) >> 10, c = idx - row * 5;
                set_m0(lds0 + L_STG + 10240 + m * 4096 + i * 1024);
                dma16p<0>(uni(src), row * 1280 + (c < 4 ? hf * 64 + c * 16 : 128 + hf * 16));
            }
            wq_fetch(wqB, sl * 5 + 2 * m, lds0 + L_STG + m * 4096);
            wq_fetch(wqB, sl * 5 + 2 * m + 1, lds0 + L_STG + m * 4096 + 2048);
            if (m) wq_fetch(wqB, sl * 5 + 4, lds0 + L_STG + 8192);
            asm volatile("s_load_dwordx2 %0, %1, 0x0\n\ts_waitcnt lgkmcnt(0)" : "=s"(tag64) : "s"(ws + OFF_EPOCH) : "memory");
        }
        tag64 += 1;
        const u32x2 tag = {(unsigned)tag64, (unsigned)(tag64 >> 32)};
        const unsigned peer = (((lane & 15) >> 1) * 32 + hp * 2 + (lane & 1)) * 128 + sl * 8;
        ST(57);
        if (w < 10) {
            DRAIN();
            if (lds_arrive(lds + L_ACNT) == 9u && lane < 48)
                gst8(ws, lane < 32 ? (unsigned)(OFF_MBX + (rb * 32 + lane) * 512 + hd * 8) : (unsigned)OFF_MBW + peer, tag);
        }
        ST(58);
        if (w == 9) {
            wait_lds<1>(lds + L_ACNT, 10, budget);
            cs_store(0);
            DRAIN();
            if (lane < 16) gst8(ws + OFF_MBC, peer, tag);
        }
        if (w >= 10) {
            const int m = w & 1, hf = hp & 1;
            DRAIN();
            u32x4 v0, v1, v2, v3, v4, v5, v[5];
            wq_read(lds0 + L_STG + m * 4096, v0, v1);
            wq_read(lds0 + L_STG + m * 4096 + 2048, v2, v3);
            wq_read(lds0 + L_STG + m * 8192, v4, v5);
            wq_sums(1, 2 * m, v0, v1);
            wq_sums(1, 2 * m + 1, v2, v3);
            if (m) wq_sums(1, 4, v4, v5);
            if (lds_arrive(lds + L_BCNT) == 1u) cs_store(1);
            char* dst = uni(ws + OFF_WO + m * WO_BYTES + (size_t)(hp >> 1) * 102400);
            const int P = min((lane >> 4) * 4 + (lane & 3), 9);
            const lds_ptr a = lds + L_STG + 10240 + m * 4096 + ((lane & 15) >> 2) * 80 + (P < 8 ? (P & 3) * 2 + (P >> 2) : P) * 8;
            const unsigned in = lane < 32 ? (2 * hf + (lane >> 4)) * 1024 + (lane & 15) * 16 : 4096 + hf * 128 + (lane - 32) * 16;
#pragma unroll
            for (int k5 = 0; k5 < 5; ++k5) {
                v[k5] = __builtin_bit_cast(u32x4, __builtin_shufflevector(lds_tr64(a + k5 * 640), lds_tr64(a + k5 * 640 + 320), 0, 1, 2, 3, 4, 5, 6, 7));
                asm volatile("" : "+v"(v[k5]));
            }
#pragma unroll
            for (int k5 = 0; k5 < 5; ++k5) {
                const int step = (sl >> 3) * 10 + (k5 == 2 ? 8 + ((sl >> 2) & 1) : sl & 7), kc = k5 == 2 ? sl & 3 : k5 - (k5 > 2);
                if (lane < 40) gst16(dst, (unsigned)(step * 5120 + kc * 256) + in, v[k5]);
            }
            wq_store(1, 2 * m, v0, v1);
            wq_store(1, 2 * m + 1, v2, v3);
            if (m) wq_store(1, 4, v4, v5);
            publish(lds0 + L_PCNT, 1u, uni(ws + OFF_MBP + (hd & ~3) * 256 + (rb * 4 + (hd & 3)) * 8), ((lane >> 2) * 32 + (lane & 3)) * 256, tag64, ~0u);
        }
        ST(59);
    }

    if (w >= 8) {
        __builtin_amdgcn_s_setprio(3);
        auto cs_frag = [&](int m) {
            const int lane = fresh_lane();
            const GAS float* cp = (const GAS float*)(ws + OFF_CS) + (m * 16 + (hd >> 1)) * 16 * 128 + (hd & 1) * 64 + lane;
            float d[16];
#pragma unroll
            for (int i = 0; i < 16; ++i) d[i] = __hip_atomic_load(cp + i * 128, __ATOMIC_RELAXED, __HIP_MEMORY_SCOPE_AGENT);
            float cs = 0.f;
#pragma unroll
            for (int i = 0; i < 16; i += 4) cs += (d[i] + d[i + 1]) + (d[i + 2] + d[i + 3]);
            const unsigned hi = pk_bf16(cs, cs);
            const unsigned lo = pk_bf16(cs - bf_lo(hi), 0.f);
            const lds_ptr f = lds + L_CS + m * 1024 + (lane >> 5) * 512 + (lane & 31) * 8;
            *(LAS u32x2*)f = (u32x2){hi, lo};
            *(LAS u32x2*)(f + 256) = (u32x2){0u, 0u};
            asm volatile("" ::: "memory");
            post_lds(lds + L_CSRDY + (lane & 3) * 4, m ? 4 : 1);
        };
        if (w == 9) {
            poll_mailbox(ws + OFF_MBC + bid * 128, (fresh_lane() & 15) * 8, tag64, 0xffffu, budget);
            cs_frag(0);
        }
        unsigned pbase = 0, rbase = 1;
#pragma unroll 1
        for (int l = 0; l < 4; ++l) {
            FRESH_SCALARS();
            const int mat = l & 1, sj = w - 8;
            const int lane = fresh_lane();
            const unsigned voff = lane * 16;
            f32x32 c0, c1; f32x16 c2;
            ST(l * 14 + 0);
            if (l == 1 && w == 9) cs_frag(1);
            unsigned g0, g1, g2;
            {
                auto gather = [](unsigned q) { const unsigned key = (q * 171) >> 9, part = q - key * 3;
                                               return (key >> 5) * 1536 + (part < 2 ? (key & 31) * 32 + part * 16 : 1024 + (key & 31) * 16); };
                g0 = gather(lane) + 2048; g1 = gather(lane + 64) + 1024; g2 = gather(lane + 128);
            }
            const char* mb = uni(ws + OFF_MBKV + (l * 32 + hd) * 8 * 128);
            const unsigned moff = ((lane >> 1) & 7) * 128 + (lane & 1) * 8;
            const char* ksrc = uni(ws + OFF_KV + l * KV_BYTES + (size_t)hd * 2 * KVQ_BYTES - 2048);
            const char* wnext = uni(ws + OFF_WQ + (mat ^ 1) * WQ_BYTES + (size_t)hd * 76800 + sj * 1024);
            const char* wosrc = uni(ws + OFF_WO + mat * WO_BYTES + (size_t)(hd >> 2) * 102400);
            const unsigned kdst = lds0 + L_RING;
            const lds_ptr prog = lds + L_PROG + (lane & 7) * 256;
            const unsigned mode = (0x19u >> (2 * l)) & 3u;
            unsigned seen = 3u << (2 * rb), n = 3 + sj;
            const lds_ptr rdy = lds + L_KVRDY + (n & 3) * 8;
            asm volatile("" : "+v"(g0), "+v"(g1), "+v"(g2));
            asm volatile("" : "+s"(mb), "+s"(ksrc));
            wait_lds<2>(lds + L_KVOUT, 4 * l + 4, budget);
            ST(l * 14 + 5);
#define STREAM(nend) kv_stream(n, (nend), seen, budget, rb, mb, ksrc, kdst, pbase, rbase, tag64, g0, g1, g2, moff, (unsigned)(uintptr_t)prog, (unsigned)(uintptr_t)rdy)
            auto prev_unit = [&]() {
                if (rb >= 1) {
                    poll_mailbox(mb, moff, tag64, 1u << (2 * rb - 2 + (sj >> 1)), budget);
                    const char* s_ = ksrc + (4 * rb - 4 + sj) * 3072;
                    set_m0(kdst + sj * 3072);
                    dma16<0>(s_, g0); dma16<1024>(s_, g1); dma16<2048>(s_, g2);
                    s_ += KVQ_BYTES;
                    set_m0(kdst + 24576 + sj * 3072);
                    dma16<0>(s_, g0); dma16<1024>(s_, g1); dma16<2048>(s_, g2);
                    DRAIN();
                }
                post_lds(lds + L_TRDY + sj * 8 + (lane & 1) * 4, rbase + 3);
                ST(l * 14 + 7);
            };
            const unsigned lastoff = voff + (sj == 3 ? 3072 : 4096);
            if (mat) {
                STREAM(0);
                if (sj == 0) post_lds(lds + L_TRDY + 32 + (lane & 1) * 4, rbase + 3);
                STREAM(0);
                wait_lds<1>(lds + L_KVOUT + 4, 4 * l + 4, budget);
                img_dma<63, 4>(wosrc, lds0 + L_WO, voff, sj, 52);
                STREAM(0);
                img_dma<63, 4>(wosrc, lds0 + L_WO, voff, 52 + sj, 100);
            } else if (sj >= 2) {
                prev_unit();
                if (l == 0) poll_mailbox(ws + OFF_MBP + bid * 256, (lane & 31) * 8, tag64, ~0u, budget);
                wait_lds<1>(lds + L_QPROG + 16 + (lane & 3) * 4, l * 16 + 2, budget);
                img_dma<63, 2>(wosrc, lds0 + L_WO, voff, 30 + sj - 2, 60);
                wait_lds<1>(lds + L_TRDY + 32 + (lane & 3) * 4, rbase, budget);
                img_dma<63, 2>(wosrc, lds0 + L_WO, voff, 60 + sj - 2, 76);
            } else {
                unsigned once = 0;
                const bool early = l == 2 || poll_mailbox(ws + OFF_MBP + bid * 256, (lane & 31) * 8, tag64, ~0u, once) == ~0u;
                if (!early) {
                    prev_unit();
                    poll_mailbox(ws + OFF_MBP + bid * 256, (lane & 31) * 8, tag64, ~0u, budget);
                }
                img_dma<63, 2>(wosrc, lds0 + L_WO, voff, 76 + sj, 100);
                wait_lds<1>(lds + L_QPROG + 16 + (lane & 3) * 4, l * 16 + 1, budget);
                img_dma<63, 2>(wosrc, lds0 + L_WO, voff, sj, 30);
                if (early) {
                    DRAIN();
                    post_lds(lds + L_WORDY + sj * 4, l + 1);
                    prev_unit();
                }
            }
            DRAIN();
            post_lds(lds + L_WORDY + sj * 4, l + 1);
            ST(l * 14 + 1);
            if (mat) STREAM(0);
            wload0(mode, c0, wnext, voff, voff);
            if (mat) STREAM(0);
            wload1(mode, c1, wnext + 32768, voff, voff);
            wload2(mode, c2, wnext + 65536, voff, lastoff);
            if (mat) STREAM(32);
#undef STREAM
            pbase += mat ? 32 : 0;
            rbase += mat ? 32 : 4;
            ST(l * 14 + 8);
            if (l < 3) {
                const lds_ptr wr = lds + L_WRDY + sj * 4;
                const unsigned nb = (l + 1) * 16, wa = lds0 + sj * 1024 + voff;
                DRAIN();
                wait_lds<2>(lds + L_WODONE + (lane & 7) * 4, l + 1, budget);
                ST(l * 14 + 11);
                if (DUMPAT == 0) wait_lds<1>(lds + L_XCNT + (lane & 1) * 4, 2u * l + 2u, budget);
                __builtin_amdgcn_s_sleep(DUMPSL);
                wdump0(c0, wa, 0); post_lds(wr, nb + 4);
                if (DUMPAT == 1) wait_lds<1>(lds + L_XCNT + (lane & 1) * 4, 2u * l + 2u, budget);
                ST(l * 14 + 12);
                wdump1(c1, wa + 32768, 0); post_lds(wr, nb + 8);
                wdump2(c2, wa + 65536, wa + 65536 - (sj == 3 ? 1024 : 0)); post_lds(wr, nb + 10);
            }
        }
        ST_FLUSH();
        return;
    }

    unsigned rbase = 1;
#pragma unroll 1
    for (int l = 0; l < 4; ++l) {
        {
            FRESH_SCALARS();
            const int mat = l & 1, qt = w < 4 ? 7 - w : w - 4, rt = mat ? qt ^ 4 : qt + 8;
            const int lane = fresh_lane(), r = lane & 31, h = lane >> 5;
            const char* xsrc = uni(ws + OFF_X + l * ACT_BYTES + (size_t)(rb * 8 + qt) * 40960);
            const char* ssrc = uni(ws + OFF_ST + l * ST_BYTES + (size_t)(rb * 16 + qt * 2) * 1024);
            char* kvd = uni(ws + OFF_KV + l * KV_BYTES + (size_t)hd * 2 * KVQ_BYTES + (rb * 8 + qt) * 1536);
            char* kvflag = uni(ws + OFF_MBKV + ((l * 32 + hd) * 8 + rb) * 128 + (qt >> 2) * 8);
            char* ob = uni(ws + OFF_O + l * ACT_BYTES + (size_t)((rb * 8 + qt) * 32 + hd) * 1280);
            char* oflag = uni(ws + OFF_MBO + (l * 256 + rb * 32 + (qt & 3)) * 512 + (qt >> 2) * 256 + hd * 8);
            unsigned ka = lds0 + L_RING + r * 48 + h * 16;
            const int vp_ = lane & 3, vquad = (lane & 16) ? 4 + min(vp_, 1) : ((vp_ & 1) * 2 + (vp_ >> 1));
            unsigned va = lds0 + L_RING + 24576 + (4 * h + ((lane & 15) >> 2)) * 48 + vquad * 8;
            unsigned a0 = lds0 + h * 960 + r * 16, a1 = lds0 + h * 960 + 512 + (r < 28 ? r : r - 12) * 16, a2 = lds0 + L_WRDY + (lane & 3) * 4;
            unsigned a3 = (r >> 4) * 1024 + h * 512 + (r & 15) * 8, a4 = ka + rt * 1536, a5 = a4 + 32 + h * 24560, a6 = lds0 + L_CS + mat * 1024 + lane * 8;
            unsigned a7 = r * 32 + h * 16, a8 = 1024 + r * 16 + h * (unsigned)KVQ_BYTES;
            float dm = (float)(r - 4 * h + mat * 64) * MASK_STEP;
            f32x16 s;
#pragma unroll
            for (int i = 0; i < 16; ++i) s[i] = fminf(dm - (float)((i & 3) + 8 * (i >> 2)) * MASK_STEP, 0.f);
            asm volatile("" : "+s"(xsrc), "+s"(ssrc), "+s"(kvd), "+s"(kvflag), "+s"(ob), "+s"(oflag));
            asm volatile("" : "+v"(a0), "+v"(a1), "+v"(a2), "+v"(a3), "+v"(a4), "+v"(a5), "+v"(a6), "+v"(a7), "+v"(a8), "+v"(ka), "+v"(va), "+v"(s), "+v"(dm));
            if (QPRIO) { if (w >= 4) __builtin_amdgcn_s_setprio(1); else __builtin_amdgcn_s_setprio(2); }
            ST(l * 14 + 0);
            wait_lds<1>(lds + L_XCNT + (w >> 2) * 4, 2u * l, budget);
            if (l == 0 && w >= 4) {
                const int sj = w - 4;
                __builtin_amdgcn_s_setprio(3);
                poll_mailbox(ws, lane < 16 ? (unsigned)(OFF_MBW + bid * 128 + lane * 8) : (unsigned)(OFF_MBX + bid * 512 + (4 * qt + (lane & 3)) * 8), tag64, 0xfffffu, budget);
                const char* wsrc = uni(ws + OFF_WQ + (size_t)hd * 76800);
                const lds_ptr wr = lds + L_WRDY + sj * 4;
                img_dma<63, 4>(wsrc, lds0, lane * 16, sj, 48);
                asm volatile("s_waitcnt vmcnt(8)" ::: "memory"); post_lds(wr, 2);
                img_dma<63, 4>(wsrc, lds0, lane * 16, 48 + sj, 72);
                img_dma<63, 4>(wsrc, lds0, lane * 16, min(72 + sj, 74), 75);
                asm volatile("s_waitcnt vmcnt(11)" ::: "memory"); post_lds(wr, 4);
                asm volatile("s_waitcnt vmcnt(7)" ::: "memory"); post_lds(wr, 6);
                asm volatile("s_waitcnt vmcnt(4)" ::: "memory"); post_lds(wr, 8);
                DRAIN(); post_lds(wr, 10);
                __builtin_amdgcn_s_setprio(0);
            } else {
                if (!XRELAY || l == 0) {
                    poll_mailbox(ws + OFF_MBX + (l * 256 + bid) * 512 + (l ? (qt >> 2) * 256 : 0), r * 8, tag64, l == 0 ? 15u << (4 * qt) : 0xffu << ((qt & 1) * 16 + (qt & 2) * 4), budget);
                } else if ((w & 2) == 0) {
                    poll_mailbox(ws + OFF_MBX + (l * 256 + bid) * 512 + (qt >> 2) * 256, r * 8, tag64, 0xffffu << ((qt & 1) * 16), budget);
                    post_lds(lds + L_QPROG + w * 4, l * 16);
                } else {
                    wait_lds<1>(lds + L_QPROG + (w - 2) * 4, l * 16, budget);
                }
                if (GATE && w < 4) wait_lds<1>(lds + L_WODONE + 16 + (lane & 3) * 4, l, budget);
            }
            ST(l * 14 + 1);
            u32x4 qm_; u32x2 qq;
            qkv_tile(qm_, qq, s, lds0 + L_TRDY + rt * 4, rbase + 3, lds0 + L_QPROG + w * 4, xsrc, ssrc, a0, a1, lane * 16, a2, a3, a4, a5, a6, l * 16, l + 1, kvd, a7, a8, budget);
            ST(l * 14 + 2);
            if (w >= 4 && ((LPMASK >> l) & 1)) __builtin_amdgcn_s_setprio(3); else __builtin_amdgcn_s_setprio(0);
            const unsigned kta = ka + 32 - h * 8;
            const s16x8 qm = __builtin_bit_cast(s16x8, qm_);
            const s16x4 qt0 = __builtin_bit_cast(s16x4, qq);
            const unsigned nm = pk_bf16(-half_swap_max(max16(s)), 0.f);
            s16x4 qtl = __builtin_bit_cast(s16x4, (u32x2){h ? nm : qq[0], qq[1]});
            f32x16 o;
            if (mat) {
                asm volatile("" : "+v"(qtl));
                publish(lds0 + L_KVOUT + (w >> 2) * 4, 4u * l + 3u, kvflag, 0u, tag64, 1u);
                wait_lds<1>(lds + L_TRDY + (lane & 1) * 4, rbase, budget);
                ST(l * 14 + 4);
                o = attn_full(qm, qtl, ka, kta, va, lds0 + L_PROG + w * 256 + lane * 4, lds0 + L_TRDY + 8 + (lane & 1) * 4, lds0 + L_KVRDY + (lane & 1) * 8, rbase, budget);
            } else {
                f32x16 cm;
                if (PUBPOS == 0) publish(lds0 + L_KVOUT + (w >> 2) * 4, 4u * l + 3u, kvflag, 0u, tag64, 1u);
                s += bf_lo(nm);
                asm volatile("" : "+v"(qtl), "+v"(s), "+v"(dm));
                if (PUBPOS == 1) publish(lds0 + L_KVOUT + (w >> 2) * 4, 4u * l + 3u, kvflag, 0u, tag64, 1u);
                asm volatile("" : "+v"(dm));
#pragma unroll
                for (int i = 0; i < 16; ++i) cm[i] = fminf((float)((i & 3) + 8 * (i >> 2) - 1) * MASK_STEP - dm, 0.f);
                asm volatile("" : "+v"(cm));
                if (PUBPOS == 2) publish(lds0 + L_KVOUT + (w >> 2) * 4, 4u * l + 3u, kvflag, 0u, tag64, 1u);
                ST(l * 14 + 4);
                o = attn_win(s, qm, qtl, cm, ka + qt * 1536, kta + qt * 1536, va + qt * 1536, lds0 + L_TRDY + qt * 4, rbase, rb ? 8 : qt, budget);
            }
            rbase += mat ? 32 : 4;
            ST(l * 14 + 7);
            auto finish = [&](const f32x16& o, float lsum) {
                const float inv = __builtin_amdgcn_rcpf(lsum);
                gst16(ob, (unsigned)(lane * 16),
                      (u32x4){pk_bf16(o[0] * inv, o[1] * inv), pk_bf16(o[2] * inv, o[3] * inv), pk_bf16(o[4] * inv, o[5] * inv), pk_bf16(o[6] * inv, o[7] * inv)});
                gst8_lo(ob, (unsigned)(1024 + lane * 8), (u32x2){pk_bf16(o[8] * inv, o[9] * inv), pk_bf16(o[10] * inv, o[11] * inv)});
            };
            auto row_sum = [](const f32x16& o) {
                auto rr = __builtin_amdgcn_permlane32_swap(__float_as_uint(o[8]), __float_as_uint(o[8]), false, false);
                return __uint_as_float(rr[1]);
            };
            const float lsum = row_sum(o);
            finish(o, lsum);
            float asum = 0.f;
#pragma unroll
            for (int i = 0; i < 12; i += 2) asum += fabsf(o[i]) + fabsf(o[i + 1]);
            const bool ok = (lsum > 5.4e-20f) && (lsum < 1.0e33f) && (asum < 1.0e38f);
            if (__builtin_expect(__builtin_amdgcn_ballot_w64(!ok) != 0, 0)) {
                wait_lds<1>(lds + L_KVOUT + (lane & 1) * 4, 4 * l + 4, budget);
                const f32x16 oe = attn_exact(!mat, ws + OFF_KV + l * KV_BYTES + (size_t)hd * 2 * KVQ_BYTES, rb * 8 + qt, qm, qt0);
                finish(oe, row_sum(oe));
            }
            ST(l * 14 + 8);
            asm volatile("s_mov_b64 exec, 1\n\tds_add_u32 %0, %1\n\ts_mov_b64 exec, 0xff\n\ts_waitcnt vmcnt(0)\n\tglobal_store_dwordx2 %2, %3, %4 sc1\n\ts_mov_b64 exec, -1"
                         :: "v"(lds0 + L_ADONE), "v"(1u), "v"(lane * 2048), "v"(tag64), "s"(oflag) : "memory");
            ST(l * 14 + 9);
        }
        {
            FRESH_SCALARS();
            const int lane = fresh_lane();
            const int up = w < 4, tt = w & 1, kh = (w >> 1) & 1, c16 = lane & 15, gq = lane >> 4;
            const int fb = hd >> 2, t32 = rb * 8 + up * 4 + (hd & 3), tin = tt * 16 + c16;
            const size_t xo = (size_t)(t32 * 80 + fb * 10) * 512;
            const unsigned xv = gq * 512 + tin * 16, xv2 = (gq >> 1) * 512 + tin * 16 + (gq & 1) * 8;
            u32x4 x0, x1; u32x2 x2;
            if (l == 0) wait_lds<1>(lds + L_TRDY + 32 + (lane & 7) * 4, 1, budget);
            {
                const char* xs = uni(ws + OFF_X + l * ACT_BYTES + xo);
                gld16<0>(x0, xs, xv); gld16<2048>(x1, xs, xv); gld8<0>(x2, xs + 4096, xv2);
            }
            wait_lds<1>(lds + L_WORDY + (lane & 3) * 4, l + 1, budget);
            const lds_ptr wp = lds + L_WO + kh * 51200 + lane * 16;
            s16x8 wf[4][5];
#pragma unroll
            for (int d = 0; d < 4; ++d)
#pragma unroll
                for (int f = 0; f < 5; ++f) wf[d][f] = lds_rd128(wp + d * 5120 + f * 1024);
            asm volatile("s_waitcnt vmcnt(0)" : "+v"(x0), "+v"(x1), "+v"(x2) :: "memory");
            f32x4 acc[5];
            {
                const unsigned keep = kh ? ~0u : 0u;
#define UNPK(a, b) (f32x4){bf_lo((a) & keep), bf_hi((a) & keep), bf_lo((b) & keep), bf_hi((b) & keep)}
                acc[0] = UNPK(x0[0], x0[1]); acc[1] = UNPK(x0[2], x0[3]); acc[2] = UNPK(x1[0], x1[1]); acc[3] = UNPK(x1[2], x1[3]); acc[4] = UNPK(x2[0], x2[1]);
#undef UNPK
            }
            const char* osrc = uni(ws + OFF_O + l * ACT_BYTES + (size_t)(t32 * 32 + kh * 16) * 1280 + 2560);
            unsigned ov = (gq >> 1) * 1280 + (gq & 1) * 512 + tin * 16, ot = gq * 2560 + 1024 + tin * 8;
            char* xd = uni(ws + OFF_X + (l + 1) * ACT_BYTES + xo + 2048);
            char* sd = uni(ws + OFF_ST + (l + 1) * ST_BYTES + ((t32 * 2 + tt) * 8 + fb) * 128);
            char* xflag = uni(ws + OFF_MBX + ((l + 1) * 256 + rb * 32) * 512 + up * 256 + ((hd & 1) * 16 + (hd & 2) * 4 + (hd >> 2)) * 8);
            unsigned exa = lds0 + L_RING + (up * 2 + tt) * 5120 + lane * 16;
            asm volatile("" : "+v"(acc[0]), "+v"(acc[1]), "+v"(acc[2]), "+v"(acc[3]), "+v"(acc[4]), "+v"(exa), "+v"(ov), "+v"(ot));
            asm volatile("" : "+s"(osrc), "+s"(xd), "+s"(sd), "+s"(xflag));
            poll_mailbox(ws + OFF_MBO + (l * 256 + bid) * 512 + up * 256 + kh * 128, c16 * 8, tag64, 0xffffu, budget);
            ST(l * 14 + 10);
            u32x4 of[8]; u32x2 og[4];
#define O_LOAD(ks) gld16<((ks) % 3) * 2560 - 2560>(of[ks], osrc + ((ks) / 3) * 7680, ov)
            O_LOAD(0); O_LOAD(1); O_LOAD(2); O_LOAD(3); O_LOAD(4); O_LOAD(5); O_LOAD(6); O_LOAD(7);
#undef O_LOAD
            gld8<-2560>(og[0], osrc, ot); gld8<-1280>(og[1], osrc, ot); gld8<0>(og[2], osrc + 7680, ot); gld8<1280>(og[3], osrc + 7680, ot);
#define WO_MMA(ks, frag) do { const s16x8 ob_ = __builtin_bit_cast(s16x8, frag); \
                _Pragma("unroll") for (int f_ = 0; f_ < 5; ++f_) acc[f_] = MFMA16(wf[(ks) & 3][f_], ob_, acc[f_]); \
                SBAR(); \
                if ((ks) < 6) { _Pragma("unroll") for (int f_ = 0; f_ < 5; ++f_) wf[(ks) & 3][f_] = lds_rd128(wp + ((ks) + 4) * 5120 + f_ * 1024); } } while (0)
#define WO_STEP(ks, N) do { asm volatile("s_waitcnt vmcnt(" #N ")" : "+v"(of[ks]) :: "memory"); WO_MMA(ks, of[ks]); } while (0)
            WO_STEP(0, 11); WO_STEP(1, 10); WO_STEP(2, 9); WO_STEP(3, 8); WO_STEP(4, 7); WO_STEP(5, 6); WO_STEP(6, 5); WO_STEP(7, 4);
            asm volatile("s_waitcnt vmcnt(0)" : "+v"(og[0]), "+v"(og[1]), "+v"(og[2]), "+v"(og[3]) :: "memory");
            WO_MMA(8, __builtin_shufflevector(og[0], og[1], 0, 1, 2, 3));
            WO_MMA(9, __builtin_shufflevector(og[2], og[3], 0, 1, 2, 3));
#undef WO_MMA
#undef WO_STEP
            LAS f32x4* ex = (LAS f32x4*)(uintptr_t)exa;
            ST(l * 14 + 11);
            if (kh == 0) {
                wait_lds<1>(lds + L_ADONE, 8 * l + 8, budget);
#pragma unroll
                for (int f = 0; f < 5; ++f) ex[f * 64] = acc[f];
                asm volatile("" ::: "memory");
                post_lds(lds + L_WODONE + w * 4, l + 1);
            } else {
                post_lds(lds + L_WODONE + w * 4, l + 1);
                wait_lds<0>(lds + L_WODONE + (w - 2) * 4, l + 1, budget);
#pragma unroll
                for (int f = 0; f < 5; ++f) acc[f] += ex[f * 64];
                const u32x4 y0 = {pk_bf16(acc[0][0], acc[0][1]), pk_bf16(acc[0][2], acc[0][3]), pk_bf16(acc[1][0], acc[1][1]), pk_bf16(acc[1][2], acc[1][3])};
                const u32x4 y1 = {pk_bf16(acc[2][0], acc[2][1]), pk_bf16(acc[2][2], acc[2][3]), pk_bf16(acc[3][0], acc[3][1]), pk_bf16(acc[3][2], acc[3][3])};
                const u32x2 y2 = {pk_bf16(acc[4][0], acc[4][1]), pk_bf16(acc[4][2], acc[4][3])};
                if (l == 3) {
                    GAS char* yo = (GAS char*)out + (size_t)(t32 * 32 + tin) * 1280 + fb * 160;
                    __builtin_nontemporal_store(y0, (GAS u32x4*)(yo + gq * 16));
                    __builtin_nontemporal_store(y1, (GAS u32x4*)(yo + 64 + gq * 16));
                    __builtin_nontemporal_store(y2, (GAS u32x2*)(yo + 128 + gq * 8));
                    if (blockIdx.x == 0 && w == 7 && lane == 0) gst8(ws + OFF_EPOCH, 0u, (u32x2){(unsigned)tag64, (unsigned)(tag64 >> 32)});
                } else {
                    asm volatile("global_store_dwordx4 %0, %2, %5 offset:-2048 sc1\n\tglobal_store_dwordx4 %0, %3, %5 sc1\n\tglobal_store_dwordx2 %1, %4, %5 offset:2048 sc1"
                                 :: "v"(xv), "v"(xv2), "v"(y0), "v"(y1), "v"(y2), "s"(xd) : "memory");
                    SBAR();
                    float s = 0.f, q = 0.f, s2 = 0.f, q2 = 0.f;
#pragma unroll
                    for (int e = 0; e < 4; ++e) {
                        const unsigned a = y0[e], c = y1[e];
                        s = dot_ones(a, s); q = dot_self(a, q); s2 = dot_ones(c, s2); q2 = dot_self(c, q2);
                    }
                    { const unsigned a = y2[0], c = y2[1]; s = dot_ones(a, s); q = dot_self(a, q); s2 = dot_ones(c, s2); q2 = dot_self(c, q2); }
                    s += s2; q += q2;
                    s = quarter_swap_add(s); q = quarter_swap_add(q);
                    s = half_swap_add(s); q = half_swap_add(q);
                    asm volatile("s_mov_b64 exec, 0xffff\n\tglobal_store_dwordx2 %0, %1, %2 sc1\n\ts_mov_b64 exec, -1"
                                 :: "v"(lane * 8), "v"((u32x2){__float_as_uint(s), __float_as_uint(q)}), "s"(sd) : "memory");
                    ST(l * 14 + 12);
                    publish(lds0 + L_XCNT + (w >> 2) * 4, 2u * l + 1u, xflag, lane * 512, tag64, ~0u);
                }
            }
            ST(l * 14 + 13);
        }
    }
    ST_FLUSH();
}

extern "C" void kernel_launch(void* const* d_in, const int* in_sizes, int n_in, void* d_out, int out_size, void* d_ws, size_t ws_size,
                              hipStream_t stream) {
    (void)in_sizes; (void)n_in; (void)out_size; (void)ws_size;
    (void)hipFuncSetAttribute((const void*)k_main, hipFuncAttributeMaxDynamicSharedMemorySize, LDS_BYTES);
    k_main<<<256, 768, LDS_BYTES, stream>>>((const char*)d_in[0], (const char*)d_in[1], (const char*)d_in[2], (const char*)d_in[4],
                                            (const char*)d_in[5], (char*)d_out, (char*)d_ws);
}
```
